```python
import torch

def _softmax(x, axis=-1):
    return torch.softmax(x, dim=axis)
def _gelu(x):
    return torch.nn.functional.gelu(x, approximate="tanh")
def _silu(x):
    return torch.nn.functional.silu(x)
def _sigmoid(x):
    return torch.sigmoid(x)
def _layernorm(x, axis=-1, eps=1e-5):
    m = x.mean(dim=axis, keepdim=True)
    v = x.var(dim=axis, keepdim=True, unbiased=False)
    return (x - m) / torch.sqrt(v + eps)
def _scatter_add(tgt, idx, src, axis=0):
    return tgt.clone().index_add_(axis, idx, src)
def _segment_sum(src, seg, n):
    out = torch.zeros((n,) + src.shape[1:], dtype=src.dtype)
    return out.index_add_(0, seg, src)
def _pad(x, pw):
    flat = tuple(v for pair in reversed(pw) for v in pair)
    return torch.nn.functional.pad(x, flat)

kernel_name = 'gated_mix__mha_block__glu_gated_geglu__prompt'

def setup_inputs():

    _w = (lambda a: (lambda t: t.to(torch.bfloat16) if t.is_floating_point() else t)(torch.as_tensor(a)))
    def _A_setup():
        B, S, D, H = 1, 2048, 512, 32
        return {
            "x": _w((lambda *s: torch.randn(*s, dtype=torch.bfloat16) * 0.05)(B, S, D)),
            "wqkv": _w((lambda *s: torch.randn(*s, dtype=torch.bfloat16) * 0.8)(D, 3 * D)),
            "wo": _w((lambda *s: torch.randn(*s, dtype=torch.bfloat16) * 0.05)(D, D)),
        }
    _A_in = _A_setup()

    def _B_setup():
        B, S, Dm, Df = 1, 2048, 512, 1792
        return {
            "x": _w((lambda *s: torch.randn(*s, dtype=torch.bfloat16) * 0.05)(B, S, Dm)),
            "w_gate": _w((lambda *s: torch.randn(*s, dtype=torch.bfloat16) * 3.2)(Dm, Df) * (Dm ** -0.5)),
            "w_up": _w((lambda *s: torch.randn(*s, dtype=torch.bfloat16) * 3.2)(Dm, Df) * (Dm ** -0.5)),
            "w_down": _w((lambda *s: torch.randn(*s, dtype=torch.bfloat16) * 3.2)(Df, Dm) * (Df ** -0.5)),
        }
    _B_in = _B_setup()

    return {
        "A_x": _A_in["x"],
        "A_wqkv": _A_in["wqkv"],
        "A_wo": _A_in["wo"],
        "B_x": _B_in["x"],
        "B_w_gate": _B_in["w_gate"],
        "B_w_up": _B_in["w_up"],
        "B_w_down": _B_in["w_down"],
        "gate_w": _w((lambda *s: torch.randn(*s, dtype=torch.bfloat16) * 0.05)(512) * 16.0),
    }

def reference(A_x, A_wqkv, A_wo, B_x, B_w_gate, B_w_up, B_w_down, gate_w):
    def _A_ref(x, wqkv, wo):
        B, S, D = x.shape
        H = 32
        Dh = D // H
        qkv = (lambda x, *d: x.permute(*d))((x @ wqkv).reshape(B, S, 3, H, Dh), 2, 0, 3, 1, 4)
        q, k, v = qkv[0], qkv[1], qkv[2]
        scores = torch.einsum("bhqd,bhkd->bhqk", q, k) * (Dh ** -0.5)
        p = _softmax(scores, axis=-1)
        o = torch.einsum("bhqk,bhkd->bhqd", p, v)
        return (lambda x, *d: x.permute(*d))(o, 0, 2, 1, 3).reshape(B, S, D) @ wo

    def _B_ref(x, w_gate, w_up, w_down):
        g = torch.einsum("bsd,df->bsf", x, w_gate)
        u = torch.einsum("bsd,df->bsf", x, w_up)
        h = _gelu(g) * u
        return torch.einsum("bsf,fd->bsd", h, w_down)

    x = A_x
    g = _sigmoid((x * gate_w).sum(-1, keepdims=True))
    a = _A_ref(x, A_wqkv, A_wo)
    b = _B_ref(x, B_w_gate, B_w_up, B_w_down)
    return g * a + (1 - g) * b
```

```cpp
#include <hip/hip_runtime.h>
#include <hip/hip_bf16.h>

typedef __attribute__((ext_vector_type(16))) float f32x16;
typedef __attribute__((ext_vector_type(8))) float f32x8;
typedef __attribute__((ext_vector_type(4))) float f32x4;
typedef __attribute__((ext_vector_type(8))) __bf16 bf16x8;
typedef __attribute__((ext_vector_type(16))) __bf16 bf16x16;
typedef __attribute__((ext_vector_type(2))) __bf16 bf16x2;
typedef __attribute__((ext_vector_type(4))) unsigned u32x4;
typedef __attribute__((ext_vector_type(2))) unsigned u32x2;
typedef __attribute__((ext_vector_type(4))) short s16x4;
typedef __attribute__((ext_vector_type(8))) short s16x8;
#define LAS __attribute__((address_space(3)))
#define GAS __attribute__((address_space(1)))
typedef LAS char* lds_p;
#define DEV __device__ __forceinline__

constexpr int LDS_BYTES = 81920;
constexpr size_t WS_Q = 0;
constexpr size_t WS_KV = 2u << 20;
constexpr size_t WS_O = 6u << 20;
constexpr size_t WS_H = 8u << 20;
constexpr size_t WS_FLAG = 16u << 20;

#define MFMA(a, b, c) __builtin_amdgcn_mfma_f32_32x32x16_bf16(a, b, c, 0, 0, 0)
#define WAITBAR(N) asm volatile("s_waitcnt vmcnt(" #N ") lgkmcnt(0)\n\ts_barrier" ::: "memory")

DEV void glds(unsigned voff, const void* sbase, unsigned ldsaddr) {
    asm volatile("s_mov_b32 m0, %2\n\ts_nop 0\n\tglobal_load_lds_dwordx4 %0, %1" ::"v"(voff), "s"(sbase), "s"(ldsaddr) : "memory");
}
DEV void glds_dev(unsigned voff, const void* sbase, unsigned ldsaddr) {
    asm volatile("s_mov_b32 m0, %2\n\ts_nop 0\n\tglobal_load_lds_dwordx4 %0, %1 sc1" ::"v"(voff), "s"(sbase), "s"(ldsaddr) : "memory");
}
DEV bf16x8 lds16(unsigned a) { return *(const LAS bf16x8*)a; }
DEV bf16x8 lds_tr(unsigned a0, unsigned a1) {
    s16x4 lo = __builtin_amdgcn_ds_read_tr16_b64_v4i16((LAS s16x4*)a0);
    s16x4 hi = __builtin_amdgcn_ds_read_tr16_b64_v4i16((LAS s16x4*)a1);
    s16x8 r = __builtin_shufflevector(lo, hi, 0, 1, 2, 3, 4, 5, 6, 7);
    return __builtin_bit_cast(bf16x8, r);
}
DEV unsigned pk(float a, float b) {
    bf16x2 r = {(__bf16)a, (__bf16)b};
    return __builtin_bit_cast(unsigned, r);
}
DEV __amdgpu_buffer_rsrc_t mkrsrc(void* p, unsigned n) { return __builtin_amdgcn_make_buffer_rsrc(p, 0, n, 0x00020000); }
DEV void st16_dev(__amdgpu_buffer_rsrc_t rs, unsigned voff, unsigned soff, u32x4 v) { __builtin_amdgcn_raw_buffer_store_b128(v, rs, voff, soff, 16); }

template <bool ISV>
DEV void qkv_loop(f32x16* acc, unsigned L, int w, int lane, int c0, const char* xs, const char* wsrc, unsigned voffA, unsigned voffB) {
    constexpr unsigned XR = 0, WR = 32768;
    const int r = lane & 31, h = lane >> 5, q = (lane & 15) >> 2, p = lane & 3, blk = (lane >> 4) & 1;
    const int g = ISV ? 2 : (w >> 1), ct = w & 1, rt0 = ISV ? ((w >> 1) & 1) : 0;
    const unsigned wa = L + WR + g * 4096 + (8 * h + q) * 128 + ((ct ^ (q >> 1)) * 64) + blk * 32 + p * 8;
    unsigned xa[4];
#pragma unroll
    for (int ks = 0; ks < 4; ++ks) xa[ks] = L + XR + (rt0 * 32 + r) * 128 + (((2 * ks + h) ^ ((r >> 1) & 7)) * 16);
    auto issue = [&](int v, int wslot) {
        const int ch = (c0 + (v >> 1)) & 7;
        const char* wb = wsrc + (size_t)((ch * 64 + (v & 1) * 32) * 3072);
        glds(voffA, wb, L + WR + wslot * 12288 + w * 1024);
        if (!ISV) glds(voffB, wb, L + WR + wslot * 12288 + (8 + w) * 1024);
        else {
            const int xo = (v + 1) >> 1, xh = (v + 1) & 1, xc = (c0 + xo) & 7;
            glds(voffB, xs + xc * 128 + xh * 32768, L + XR + (xo & 3) * 8192 + (xh * 4 + (w - 4)) * 1024);
        }
    };
    if (ISV) glds(voffB, xs + c0 * 128, L + XR + (w - 4) * 1024);
    issue(0, 0); issue(1, 1); issue(2, 2);
    auto step = [&](int j, unsigned xoff) {
#pragma unroll
        for (int kk = 0; kk < 2; ++kk) {
            const unsigned wo = j * 12288 + kk * 2048;
            bf16x8 wf = lds_tr(wa + wo, wa + wo + 512);
            const unsigned xo = xoff + (j >> 1) * 8192;
            bf16x8 x0 = lds16(xa[2 * (j & 1) + kk] + xo);
            if (ISV) acc[0] = MFMA(x0, wf, acc[0]);
            else {
                bf16x8 x1 = lds16(xa[2 * (j & 1) + kk] + xo + 4096);
                acc[0] = MFMA(wf, x0, acc[0]);
                acc[1] = MFMA(wf, x1, acc[1]);
            }
        }
    };
#pragma unroll 1
    for (int it = 0; it < 3; ++it) {
        const unsigned xoff = (it & 1) * 16384;
#pragma unroll
        for (int j = 0; j < 4; ++j) {
            WAITBAR(4);
            issue(4 * it + j + 3, (j + 3) & 3);
            step(j, xoff);
        }
    }
    WAITBAR(4); issue(15, 3); step(0, 16384);
    WAITBAR(4); step(1, 16384);
    WAITBAR(2); step(2, 16384);
    WAITBAR(0); step(3, 16384);
}

DEV void qkv_phase(int b, int w, int lane, unsigned L, const char* x, const char* wqkv, char* ws) {
    const int xcd = b & 7, i = b >> 3;
    const int rb = (xcd >> 1) * 8 + (i & 7), cb = (xcd & 1) * 4 + (i >> 3), c0 = (2 * (i >> 3) + (i & 7)) & 7;
    const char* xs = x + (size_t)rb * 65536;
    const char* wsrc = wqkv + cb * 128;
    const int kA = 8 * (w & 3) + (lane >> 3);
    const unsigned wsw = ((lane & 7) ^ (((kA >> 1) & 1) << 2)) * 16;
    const unsigned voffA = kA * 3072 + (w >> 2) * 1024 + wsw;
    const int r = lane & 31, h = lane >> 5;
    __amdgpu_buffer_rsrc_t rs = mkrsrc(ws, (unsigned)WS_O);
    if (w < 4) {
        f32x16 acc[2] = {};
        qkv_loop<false>(acc, L, w, lane, c0, xs, wsrc, voffA, kA * 3072 + 2048 + wsw);
        const float sc = w < 2 ? 0.25f * 1.4426950408889634f : 1.0f;
        const int rk = w < 2 ? r : ((r & 19) | ((r & 4) << 1) | ((r & 8) >> 1));
        const unsigned vo = (h * 32 + rk) * 16;
#pragma unroll
        for (int rt = 0; rt < 2; ++rt)
#pragma unroll
            for (int s = 0; s < 2; ++s) {
                const int head = cb * 4 + (w & 1) * 2 + s, tile = rb * 2 + rt;
                const unsigned so = w < 2 ? (unsigned)WS_Q + (head * 64 + tile) * 1024
                                          : (unsigned)WS_KV + head * 131072 + (tile >> 3) * 16384 + (tile & 7) * 1024;
                u32x4 v;
#pragma unroll
                for (int j = 0; j < 4; ++j) v[j] = pk(acc[rt][8 * s + 2 * j] * sc, acc[rt][8 * s + 2 * j + 1] * sc);
                st16_dev(rs, vo, so, v);
            }
    } else {
        f32x16 acc[1] = {};
        qkv_loop<true>(acc, L, w, lane, c0, xs, wsrc, voffA, kA * 1024 + (((lane & 7) ^ ((kA >> 1) & 7)) * 16));
        const int tile = rb * 2 + ((w >> 1) & 1);
        const unsigned vo = (r >> 4) * 131072 + (h * 16 + (r & 15)) * 16;
        const unsigned so = (unsigned)WS_KV + (cb * 4 + (w & 1) * 2) * 131072 + (tile >> 3) * 16384 + 8192 + (tile & 7) * 1024;
#pragma unroll
        for (int s = 0; s < 2; ++s) {
            u32x4 v;
#pragma unroll
            for (int j = 0; j < 4; ++j) v[j] = pk(acc[0][8 * s + 2 * j], acc[0][8 * s + 2 * j + 1]);
            st16_dev(rs, vo + s * 512, so, v);
        }
    }
}

DEV f32x16 pv(bf16x8 v, f32x8 p, f32x16 o, int idx) {
    return __builtin_amdgcn_smfmac_f32_32x32x32_bf16(v, __builtin_bit_cast(bf16x16, p), o, idx, 0, 0);
}
DEV float half_max(float m) {
    auto rr = __builtin_amdgcn_permlane32_swap(__float_as_uint(m), __float_as_uint(m), false, false);
    return __builtin_fmaxf(__uint_as_float(rr[0]), __uint_as_float(rr[1]));
}
DEV float max16(const f32x16& s) {
    float m = s[0];
#pragma unroll
    for (int i = 1; i < 16; ++i) m = __builtin_fmaxf(m, s[i]);
    return m;
}
DEV void att_tile(f32x16& s, bf16x8 v0, bf16x8 v1, f32x16& o, int idx) {
    f32x8 p0, p1;
#pragma unroll
    for (int i = 0; i < 8; ++i) p0[i] = __builtin_amdgcn_exp2f(s[i]);
#pragma unroll
    for (int i = 0; i < 8; ++i) p1[i] = __builtin_amdgcn_exp2f(s[8 + i]);
    o = pv(v0, p0, o, idx);
    o = pv(v1, p1, o, idx);
}

DEV void attn_phase(int b, int w, int lane, unsigned L, char* ws) {
    const int xcd = b & 7, i = b >> 3, head = xcd * 4 + (i >> 3), qb = i & 7;
    const int r = lane & 31, h = lane >> 5;
    constexpr unsigned ONES = 65536;
    const char* kv = ws + WS_KV + (size_t)head * 131072;
    const unsigned voff = w * 2048 + lane * 16;
    auto issue = [&](int c) {
        const char* src = kv + ((qb + c) & 7) * 16384;
        glds_dev(voff, src, L + (c & 3) * 16384 + w * 2048);
        glds_dev(voff, src + 1024, L + (c & 3) * 16384 + w * 2048 + 1024);
    };
    __amdgpu_buffer_rsrc_t rs = mkrsrc(ws, (unsigned)WS_H);
    const int tile = qb * 8 + w;
    bf16x8 qf = __builtin_bit_cast(bf16x8, __builtin_amdgcn_raw_buffer_load_b128(rs, lane * 16, (unsigned)WS_Q + (head * 64 + tile) * 1024, 16));
    asm volatile("" : "+v"(qf));
    issue(0); issue(1); issue(2); issue(3);
    *(LAS u32x4*)(L + ONES + (w * 64 + lane) * 16) = u32x4{0x3F803F80u, 0x3F803F80u, 0x3F803F80u, 0x3F803F80u};
    const int idx = 0xDDDD;
    unsigned ka = L + lane * 16;
    unsigned va = r < 16 ? L + 8192 + (h * 16 + r) * 16 : L + ONES + (r - 16) * 16;
    const unsigned vinc = r < 16 ? 16384u : 0u;
    WAITBAR(6);
    f32x16 zero = {};
    f32x16 s0 = MFMA(lds16(ka), qf, zero);
    const float nm = -half_max(max16(s0));
    f32x16 ci;
#pragma unroll
    for (int j = 0; j < 16; ++j) ci[j] = nm;
    asm volatile("" : "+v"(ci));
    f32x16 o = {};
    f32x16 sa = MFMA(lds16(ka), qf, ci), sb;
#pragma unroll 1
    for (int c = 0; c < 8; ++c) {
        const unsigned kc = ka + (c & 3) * 16384, vc = va + (c & 3) * vinc;
        const unsigned kn = ka + ((c + 1) & 3) * 16384;
#pragma unroll
        for (int t = 0; t < 8; t += 2) {
            sb = MFMA(lds16(kc + (t + 1) * 1024), qf, ci);
            att_tile(sa, lds16(vc + t * 1024), lds16(vc + t * 1024 + 512), o, idx);
            if (t == 6) {
                if (c < 5) { WAITBAR(2); } else { WAITBAR(0); }
                if (c >= 1 && c < 5) issue(c + 3);
                sa = MFMA(lds16(kn), qf, ci);
            } else {
                sa = MFMA(lds16(kc + (t + 2) * 1024), qf, ci);
            }
            att_tile(sb, lds16(vc + (t + 1) * 1024), lds16(vc + (t + 1) * 1024 + 512), o, idx);
        }
    }
    float osum = 0.f;
#pragma unroll
    for (int j = 0; j < 8; ++j) osum += __builtin_fabsf(o[j]);
    const bool ok = o[8] <= 1.8e19f && osum <= 1e37f;
    if (!__all(ok)) {
        const unsigned vo = r < 16 ? 8192 + (h * 16 + r) * 16 : 0;
        const unsigned kb = (unsigned)WS_KV + head * 131072;
        float m = -INFINITY;
#pragma unroll 1
        for (int t = 0; t < 64; ++t) {
            bf16x8 kf = __builtin_bit_cast(bf16x8, __builtin_amdgcn_raw_buffer_load_b128(rs, lane * 16, kb + (t >> 3) * 16384 + (t & 7) * 1024, 16));
            m = __builtin_fmaxf(m, max16(MFMA(kf, qf, zero)));
        }
        m = -half_max(m);
#pragma unroll
        for (int j = 0; j < 16; ++j) { ci[j] = m; o[j] = 0.f; }
        const u32x4 one4 = {0x3F803F80u, 0x3F803F80u, 0x3F803F80u, 0x3F803F80u};
#pragma unroll 1
        for (int t = 0; t < 64; ++t) {
            const unsigned so = kb + (t >> 3) * 16384 + (t & 7) * 1024;
            bf16x8 kf = __builtin_bit_cast(bf16x8, __builtin_amdgcn_raw_buffer_load_b128(rs, lane * 16, so, 16));
            u32x4 v0 = __builtin_amdgcn_raw_buffer_load_b128(rs, vo, so, 16), v1 = __builtin_amdgcn_raw_buffer_load_b128(rs, vo + 512, so, 16);
            if (r >= 16) { v0 = one4; v1 = one4; }
            f32x16 s = MFMA(kf, qf, ci);
            att_tile(s, __builtin_bit_cast(bf16x8, v0), __builtin_bit_cast(bf16x8, v1), o, idx);
        }
    }
    const float rl = 1.0f / o[8];
    u32x4 v;
#pragma unroll
    for (int j = 0; j < 4; ++j) v[j] = pk(o[2 * j] * rl, o[2 * j + 1] * rl);
    st16_dev(rs, lane * 16, (unsigned)WS_O + (tile * 32 + head) * 1024, v);
}

DEV float geglu(float g, float u) {
    const float c0 = -2.3022081985f, c1 = c0 * 0.044715f;
    const float e = __builtin_amdgcn_exp2f(g * __builtin_fmaf(g * g, c1, c0));
    return g * u * __builtin_amdgcn_rcpf(1.0f + e);
}
DEV void gateup_phase(int b, int w, int lane, unsigned L, const char* x, const char* wg, const char* wu, char* ws) {
    const int xcd = b & 7, i = b >> 3;
    if (i >= 28) return;
    const int rb = (xcd >> 1) * 4 + (i & 3), cb = (xcd & 1) * 7 + (i >> 2), c0 = (2 * (i & 3) + (i >> 2)) & 7;
    constexpr unsigned XR = 0, WR = 32768;
    const int r = lane & 31, h = lane >> 5, q = (lane & 15) >> 2, p = lane & 3, up = (lane >> 4) & 1;
    const int wr = w >> 2, wc = w & 3;
    const char* xs = x + (size_t)rb * 131072;
    const char* gs = wg + cb * 256;
    const char* us = wu + cb * 256;
    const int kq = lane >> 4, i16 = lane & 15;
    const unsigned voffG = (4 * w + kq) * 3584 + ((((i16 >> 1) ^ kq) << 1) | (i16 & 1)) * 16;
    const unsigned voffU = voffG ^ 128;
    const int xrow = 8 * w + (lane >> 3);
    const unsigned voffX = xrow * 1024 + (((lane & 7) ^ ((xrow >> 1) & 7)) * 16);
    auto issueW = [&](int v, int slot) {
        const int ch = (c0 + (v >> 1)) & 7;
        const size_t o = (size_t)((ch * 64 + (v & 1) * 32) * 3584);
        glds(voffG, gs + o, L + WR + slot * 16384 + w * 1024);
        glds(voffU, us + o, L + WR + slot * 16384 + 8192 + w * 1024);
    };
    auto issueX = [&](int o) {
        const char* s = xs + ((c0 + o) & 7) * 128;
        glds(voffX, s, L + XR + (o & 1) * 16384 + w * 1024);
        glds(voffX, s + 65536, L + XR + (o & 1) * 16384 + 8192 + w * 1024);
    };
    unsigned wa[2], xa[4];
#pragma unroll
    for (int c = 0; c < 2; ++c) wa[c] = L + WR + up * 8192 + (8 * h + q) * 256 + (((wc * 2 + c) ^ (q | (up << 2))) * 32) + p * 8;
#pragma unroll
    for (int ks = 0; ks < 4; ++ks) xa[ks] = L + XR + (wr * 64 + r) * 128 + (((2 * ks + h) ^ ((r >> 1) & 7)) * 16);
    f32x16 acc[2][2] = {};
    auto step = [&](int par, unsigned woff, unsigned xoff) {
#pragma unroll
        for (int kk = 0; kk < 2; ++kk) {
            bf16x8 w0 = lds_tr(wa[0] + woff + kk * 4096, wa[0] + woff + kk * 4096 + 1024);
            bf16x8 w1 = lds_tr(wa[1] + woff + kk * 4096, wa[1] + woff + kk * 4096 + 1024);
            bf16x8 x0 = lds16(xa[2 * par + kk] + xoff), x1 = lds16(xa[2 * par + kk] + xoff + 4096);
            acc[0][0] = MFMA(w0, x0, acc[0][0]);
            acc[0][1] = MFMA(w1, x0, acc[0][1]);
            acc[1][0] = MFMA(w0, x1, acc[1][0]);
            acc[1][1] = MFMA(w1, x1, acc[1][1]);
        }
    };
    issueX(0); issueW(0, 0); issueW(1, 1);
    int s0 = 0, s1 = 1, s2 = 2;
#pragma unroll 1
    for (int it = 0; it < 7; ++it) {
        const unsigned xoff = (it & 1) * 16384;
        WAITBAR(2);
        issueW(2 * it + 2, s2); issueX(it + 1);
        step(0, s0 * 16384, xoff);
        WAITBAR(4);
        issueW(2 * it + 3, s0);
        step(1, s1 * 16384, xoff);
        const int t = s0; s0 = s2; s2 = s1; s1 = t;
    }
    WAITBAR(2); step(0, s0 * 16384, 16384);
    WAITBAR(0); step(1, s1 * 16384, 16384);
    __amdgpu_buffer_rsrc_t rs = mkrsrc(ws + WS_H, 64u * 112u * 1024u);
#pragma unroll
    for (int rt = 0; rt < 2; ++rt)
#pragma unroll
        for (int c = 0; c < 2; ++c) {
            u32x4 v;
#pragma unroll
            for (int j = 0; j < 4; ++j)
                v[j] = pk(geglu(acc[rt][c][2 * j], acc[rt][c][8 + 2 * j]), geglu(acc[rt][c][2 * j + 1], acc[rt][c][8 + 2 * j + 1]));
            st16_dev(rs, lane * 16, ((rb * 4 + wr * 2 + rt) * 112 + cb * 8 + wc * 2 + c) * 1024, v);
        }
}

DEV void final_phase(int b, int w, int lane, unsigned L, const char* x, const char* wo, const char* wd, const char* gw, char* ws, char* out) {
    const int xcd = b & 7, i = b >> 3;
    const int rb = (xcd >> 1) * 8 + (i & 7), cb = (xcd & 1) * 4 + (i >> 3);
    const int r = lane & 31, h = lane >> 5, q = (lane & 15) >> 2, p = lane & 3, blk = (lane >> 4) & 1;
    const int tid = w * 64 + lane;
    const int kr = lane >> 3;
    const unsigned voffW = kr * 1024 + (((lane & 7) ^ (((kr >> 1) & 1) << 2)) * 16);
    const unsigned wtr = (4 * h + q) * 128 + ((q >> 1) * 64) + blk * 32 + p * 8;
    float gate[2];
    {
        const u32x4* gp = (const u32x4*)gw + (lane & 15);
#pragma unroll
        for (int rr = 0; rr < 2; ++rr) {
            const u32x4* xp = (const u32x4*)(x + (size_t)(rb * 64 + rr * 32 + (tid >> 4)) * 1024) + (lane & 15);
            float d = 0.f;
#pragma unroll
            for (int j = 0; j < 4; ++j) {
                const u32x4 a = xp[16 * j], c = gp[16 * j];
#pragma unroll
                for (int e = 0; e < 4; ++e) { const unsigned ae = a[e], ce = c[e]; d = __builtin_amdgcn_fdot2_f32_bf16(__builtin_bit_cast(bf16x2, ae), __builtin_bit_cast(bf16x2, ce), d, false); }
            }
#pragma unroll
            for (int m = 1; m < 16; m <<= 1) d += __shfl_xor(d, m);
            gate[rr] = __builtin_amdgcn_rcpf(1.0f + __builtin_amdgcn_exp2f(-1.4426950408889634f * d));
        }
        asm volatile("" : "+v"(gate[0]), "+v"(gate[1]));
    }
    f32x4 bsum[2];
    {
        const unsigned P = L + w * 8192;
        const char* hs = ws + WS_H + (size_t)(rb * 2 * 112 + w * 14) * 1024;
        const char* wsrc = wd + (size_t)(w * 224) * 1024 + cb * 128;
        const unsigned l16 = lane * 16;
        auto issue = [&](int s) {
            const unsigned d = P + (s & 1) * 4096;
            glds(voffW, wsrc + s * 16384, d);
            glds(voffW, wsrc + s * 16384 + 8192, d + 1024);
            glds_dev(l16, hs + s * 1024, d + 2048);
            glds_dev(l16, hs + s * 1024 + 112 * 1024, d + 3072);
        };
        f32x16 acc[2][2] = {};
        issue(0); issue(1);
        auto step = [&](int s, int par, bool more) {
            const unsigned d = P + par * 4096;
            bf16x8 w0 = lds_tr(d + wtr, d + wtr + 1024), w1 = lds_tr(d + (wtr ^ 64), d + (wtr ^ 64) + 1024);
            bf16x8 h0 = lds16(d + 2048 + l16), h1 = lds16(d + 3072 + l16);
            asm volatile("s_waitcnt lgkmcnt(0)" : "+v"(w0), "+v"(w1), "+v"(h0), "+v"(h1)::"memory");
            if (more) issue(s + 2);
            acc[0][0] = MFMA(w0, h0, acc[0][0]);
            acc[0][1] = MFMA(w1, h0, acc[0][1]);
            acc[1][0] = MFMA(w0, h1, acc[1][0]);
            acc[1][1] = MFMA(w1, h1, acc[1][1]);
        };
#pragma unroll 1
        for (int s = 0; s < 12; s += 2) {
            asm volatile("s_waitcnt vmcnt(4)" ::: "memory"); step(s, 0, true);
            asm volatile("s_waitcnt vmcnt(4)" ::: "memory"); step(s + 1, 1, true);
        }
        asm volatile("s_waitcnt vmcnt(4)" ::: "memory"); step(12, 0, false);
        asm volatile("s_waitcnt vmcnt(0)" ::: "memory"); step(13, 1, false);
        const unsigned rd = L + (tid >> 4) * 256 + (((tid & 15) ^ ((tid >> 4) & 7)) * 16);
#pragma unroll
        for (int rt = 0; rt < 2; ++rt) {
            if (rt == 1) WAITBAR(0);
#pragma unroll
            for (int c = 0; c < 2; ++c)
#pragma unroll
                for (int g = 0; g < 4; ++g) {
                    f32x4 v = {acc[rt][c][4 * g], acc[rt][c][4 * g + 1], acc[rt][c][4 * g + 2], acc[rt][c][4 * g + 3]};
                    *(LAS f32x4*)(P + r * 256 + (((c * 8 + 2 * g + h) ^ (r & 7)) * 16)) = v;
                }
            WAITBAR(0);
            f32x4 s = *(LAS f32x4*)rd;
#pragma unroll
            for (int k = 1; k < 8; ++k) s += *(LAS f32x4*)(rd + k * 8192);
            bsum[rt] = s;
        }
    }
    const int rt = w & 1, ct = (w >> 1) & 1, kh = w >> 2;
    WAITBAR(0);
    {
        const char* wsrc = wo + (size_t)(w * 64) * 1024 + cb * 128;
#pragma unroll
        for (int j = 0; j < 8; ++j) glds(voffW, wsrc + j * 8192, L + w * 8192 + j * 1024);
    }
    WAITBAR(0);
    bf16x8 wf[16];
    {
        const unsigned a = L + kh * 32768 + (wtr ^ (ct * 64));
#pragma unroll
        for (int j = 0; j < 16; ++j) wf[j] = lds_tr(a + j * 2048, a + j * 2048 + 1024);
    }
    WAITBAR(0);
    {
        const char* os = ws + WS_O + (size_t)(rb * 64 + w * 8) * 1024;
#pragma unroll
        for (int j = 0; j < 8; ++j) glds_dev(lane * 16, os + j * 1024, L + w * 8192 + j * 1024);
    }
    WAITBAR(0);
    f32x16 acc = {};
    {
        const unsigned a = L + (rt * 32 + kh * 16) * 1024 + lane * 16;
#pragma unroll
        for (int j = 0; j < 16; ++j) acc = MFMA(wf[j], lds16(a + j * 1024), acc);
    }
    WAITBAR(0);
#pragma unroll
    for (int g = 0; g < 4; ++g) {
        f32x4 v = {acc[4 * g], acc[4 * g + 1], acc[4 * g + 2], acc[4 * g + 3]};
        *(LAS f32x4*)(L + kh * 16384 + (rt * 32 + r) * 256 + (((ct * 8 + 2 * g + h) ^ (r & 7)) * 16)) = v;
    }
    WAITBAR(0);
    const unsigned rd = L + (tid >> 4) * 256 + (((tid & 15) ^ ((tid >> 4) & 7)) * 16);
#pragma unroll
    for (int rr = 0; rr < 2; ++rr) {
        const f32x4 a = *(LAS f32x4*)(rd + rr * 8192) + *(LAS f32x4*)(rd + rr * 8192 + 16384);
        const f32x4 o = bsum[rr] + gate[rr] * (a - bsum[rr]);
        u32x2 v = {pk(o[0], o[1]), pk(o[2], o[3])};
        *(GAS u32x2*)(out + (size_t)(rb * 64 + rr * 32 + (tid >> 4)) * 1024 + cb * 128 + (tid & 15) * 8) = v;
    }
}

#define PRE                                                             \
    extern __shared__ __attribute__((aligned(16))) char smem[];        \
    const unsigned L = (unsigned)(size_t)(lds_p)smem;                   \
    const int w = __builtin_amdgcn_readfirstlane(threadIdx.x >> 6), lane = threadIdx.x & 63

__global__ __launch_bounds__(512, 4) void k_qkv(const char* x, const char* wqkv, char* ws) { PRE; qkv_phase(blockIdx.x, w, lane, L, x, wqkv, ws); }
__global__ __launch_bounds__(512, 4) void k_attn(char* ws) { PRE; attn_phase(blockIdx.x, w, lane, L, ws); }
__global__ __launch_bounds__(512, 4) void k_gateup(const char* x, const char* wg, const char* wu, char* ws) { PRE; gateup_phase(blockIdx.x, w, lane, L, x, wg, wu, ws); }
__global__ __launch_bounds__(512, 4) void k_final(const char* x, const char* wo, const char* wd, const char* gw, char* ws, char* out) { PRE; final_phase(blockIdx.x, w, lane, L, x, wo, wd, gw, ws, out); }

extern "C" void kernel_launch(void* const* d_in, const int* in_sizes, int n_in, void* d_out, int out_size, void* d_ws, size_t ws_size,
                              hipStream_t stream) {
    const char* x = (const char*)d_in[0];
    const char* wqkv = (const char*)d_in[1];
    const char* wo = (const char*)d_in[2];
    const char* wg = (const char*)d_in[4];
    const char* wu = (const char*)d_in[5];
    const char* wd = (const char*)d_in[6];
    const char* gw = (const char*)d_in[7];
    char* ws = (char*)d_ws;
    (void)hipFuncSetAttribute((const void*)k_qkv, hipFuncAttributeMaxDynamicSharedMemorySize, LDS_BYTES);
    (void)hipFuncSetAttribute((const void*)k_attn, hipFuncAttributeMaxDynamicSharedMemorySize, LDS_BYTES);
    (void)hipFuncSetAttribute((const void*)k_gateup, hipFuncAttributeMaxDynamicSharedMemorySize, LDS_BYTES);
    (void)hipFuncSetAttribute((const void*)k_final, hipFuncAttributeMaxDynamicSharedMemorySize, LDS_BYTES);
    k_qkv<<<256, 512, LDS_BYTES, stream>>>(x, wqkv, ws);
    k_gateup<<<256, 512, LDS_BYTES, stream>>>(x, wg, wu, ws);
    k_attn<<<256, 512, LDS_BYTES, stream>>>(ws);
    k_final<<<256, 512, LDS_BYTES, stream>>>(x, wo, wd, gw, ws, (char*)d_out);
}
```

```cpp
#include <hip/hip_runtime.h>
#include <hip/hip_bf16.h>

constexpr unsigned WS_USED = 17u << 20;

struct Desc { const void* p; unsigned bytes, flags; };
struct Args { Desc d[9]; };

__global__ __launch_bounds__(512, 2) void gated_mix_fused(Args) {
    __shared__ __attribute__((aligned(16))) char smem[163840];
    unsigned tid = threadIdx.x, bid = blockIdx.x;
    auto args = __builtin_amdgcn_kernarg_segment_ptr();
    asm volatile(
        "s_getpc_b64 s[92:93]\n"
        "LBEG%=:\n"
        "s_lshr_b32 s79, s2, 6\n"
        "s_cmp_eq_u32 s79, 3\n"
        "s_cselect_b32 s57, 16, 32\n"
        "s_cselect_b32 s58, 16, 48\n"
        "s_load_dwordx16 s[4:19], s[0:1], 0x0\n"
        "s_load_dwordx4 s[40:43], s[0:1], s57\n"
        "s_load_dwordx4 s[44:47], s[0:1], s58\n"
        "s_load_dwordx16 s[20:35], s[0:1], 0x40\n"
        "s_load_dwordx4 s[36:39], s[0:1], 0x80\n"
        "s_and_b32 s59, s2, 0xf8\n"
        "v_lshrrev_b32 v1, 6, v0\n"
        "s_mov_b32 m0, 155648\n"
        "v_lshrrev_b32 v2, 5, v0\n"
        "v_lshl_add_u32 v2, s59, 1, v2\n"
        "v_mul_u32_u24 v2, 64, v2\n"
        "v_add_u32 v2, LEND%=-LBEG%=-32704, v2\n"
        "global_load_lds_dword v2, s[92:93]\n"
        "v_readfirstlane_b32 s52, v1\n"
        "s_and_b32 s78, s2, 7\n"
        "s_bfe_u32 s80, s2, 0x30003\n"
        "s_and_b32 s59, s2, 3\n"
        "s_and_b32 s60, s2, 4\n"
        "s_lshl_b32 s60, s60, 1\n"
        "s_add_u32 s81, s60, s80\n"
        "s_lshl_b32 s82, s59, 2\n"
        "s_add_u32 s82, s82, s79\n"
        "s_add_u32 s55, s80, s79\n"
        "s_and_b32 s3, s55, 1\n"
        "s_and_b32 s95, s55, 3\n"
        "s_add_u32 s55, s55, s79\n"
        "s_and_b32 s55, s55, 7\n"
        "s_lshl_b32 s53, s52, 10\n"
        "s_lshl_b32 s77, s59, 1\n"
        "s_and_b32 s60, s2, 4\n"
        "s_xor_b32 s77, s77, s60\n"
        "s_xor_b32 s3, s3, s77\n"
        "s_xor_b32 s95, s95, s77\n"
        "s_mul_i32 s60, s59, 3\n"
        "s_add_u32 s60, s60, s79\n"
        "s_lshl_b32 s59, s59, 1\n"
        "s_cmp_eq_u32 s79, 3\n"
        "s_cselect_b32 s63, s59, s60\n"
        "s_cselect_b32 s56, 6, 7\n"
        "s_cselect_b32 s60, 0x80, 0\n"
        "s_add_u32 s63, s63, 16\n"
        "s_lshl_b32 s59, s63, 7\n"
        "s_lshl_b32 s56, s56, 9\n"
        "v_lshrrev_b32 v1, 3, v0\n"
        "v_and_b32 v2, 7, v0\n"
        "v_lshrrev_b32 v3, 1, v1\n"
        "v_and_b32 v3, 4, v3\n"
        "v_and_or_b32 v3, v1, 2, v3\n"
        "v_xor_b32 v3, v3, v2\n"
        "v_lshlrev_b32 v3, 4, v3\n"
        "v_bfe_u32 v4, v0, 4, 3\n"
        "v_xor_b32 v4, v4, v2\n"
        "v_lshlrev_b32 v4, 4, v4\n"
        "v_lshl_add_u32 v4, v1, 10, v4\n"
        "s_lshl_b32 s57, s81, 17\n"
        "v_add_u32 v104, s57, v4\n"
        "s_lshl_b32 s57, s82, 7\n"
        "v_add_u32 v5, s57, v3\n"
        "s_movk_i32 s58, 0xc00\n"
        "v_mad_u32_u24 v105, v1, s58, v5\n"
        "s_movk_i32 s58, 0xe00\n"
        "v_mad_u32_u24 v108, v1, s58, v5\n"
        "v_add_u32 v5, s59, v3\n"
        "v_mad_u32_u24 v106, v1, s56, v5\n"
        "v_add_u32 v107, s60, v106\n"
        "s_lshl_b32 s56, s56, 6\n"
        "s_mov_b32 s59, 16777216\n"
        "s_waitcnt lgkmcnt(0)\n"
        "s_load_dwordx2 s[48:49], s[28:29], s59\n"
        "s_mov_b32 m0, s53\n"
        "s_xor_b32 s57, s3, 0\n"
        "s_lshl_b32 s58, s57, 7\n"
        "buffer_load_dwordx4 v104, s[4:7], s58 offen lds\n"
        "s_add_u32 m0, s53, 8192\n"
        "s_add_u32 s58, s58, 0x10000\n"
        "buffer_load_dwordx4 v104, s[4:7], s58 offen lds\n"
        "s_add_u32 m0, s53, 49152\n"
        "s_mul_i32 s58, s57, 196608\n"
        "buffer_load_dwordx4 v105, s[8:11], s58 offen lds\n"
        "s_add_u32 m0, s53, 73728\n"
        "s_mul_i32 s58, s57, s56\n"
        "buffer_load_dwordx4 v106, s[40:43], s58 offen lds\n"
        "s_add_u32 m0, s53, 81920\n"
        "s_nop 0\n"
        "buffer_load_dwordx4 v107, s[44:47], s58 offen lds\n"
        "s_lshr_b32 s83, s52, 2\n"
        "s_and_b32 s84, s52, 3\n"
        "v_and_b32 v1, 63, v0\n"
        "v_lshlrev_b32 v124, 4, v1\n"
        "v_lshrrev_b32 v2, 4, v1\n"
        "v_bfe_u32 v3, v1, 2, 2\n"
        "v_lshl_add_u32 v4, v2, 3, v3\n"
        "v_and_b32 v5, 3, v1\n"
        "v_lshlrev_b32 v5, 3, v5\n"
        "v_lshl_add_u32 v4, v4, 7, v5\n"
        "v_bfe_u32 v6, v1, 3, 2\n"
        "s_lshl_b32 s57, s83, 1\n"
        "v_xor_b32 v6, s57, v6\n"
        "v_lshl_add_u32 v116, v6, 5, v4\n"
        "v_xor_b32 v117, 32, v116\n"
        "v_add_u32 v118, 49152, v116\n"
        "v_add_u32 v119, 49152, v117\n"
        "v_add_u32 v120, 98304, v116\n"
        "v_add_u32 v121, 98304, v117\n"
        "v_and_b32 v3, 15, v1\n"
        "s_lshl_b32 s57, s84, 12\n"
        "v_lshlrev_b32 v4, 7, v3\n"
        "v_add_u32 v4, s57, v4\n"
        "v_lshrrev_b32 v3, 1, v3\n"
        "v_xor_b32 v3, v3, v2\n"
        "v_lshl_add_u32 v110, v3, 4, v4\n"
        "v_xor_b32 v111, 64, v110\n"
        "v_mov_b32 v122, 0xc0135761\n"
        "v_xor_b32 v62, 0x1000, v110\n"
        "v_xor_b32 v63, 0x1000, v111\n"
        "s_and_b32 s57, s52, 1\n"
        "s_lshl_b32 s57, s57, 5\n"
        "v_xor_b32 v48, s57, v118\n"
        "v_xor_b32 v49, s57, v120\n"
        "s_lshl_b32 s54, s52, 11\n"
        "s_lshl_b32 s85, s78, 2\n"
        "s_add_u32 s85, s85, s79\n"
        "s_and_b32 s92, s78, 6\n"
        "s_lshl_b32 s92, s92, 2\n"
        "s_lshl_b32 s93, s79, 1\n"
        "s_add_u32 s92, s92, s93\n"
        "s_and_b32 s93, s80, 1\n"
        "s_add_u32 s86, s92, s93\n"
        "s_and_b32 s92, s78, 1\n"
        "s_lshl_b32 s92, s92, 2\n"
        "s_lshr_b32 s93, s80, 1\n"
        "s_add_u32 s87, s92, s93\n"
        "s_and_b32 s88, s52, 1\n"
        "s_lshr_b32 s89, s52, 1\n"
        "s_lshl_b32 s59, s81, 2\n"
        "s_add_u32 s59, s59, s84\n"
        "s_lshr_b32 s60, s59, 3\n"
        "s_lshl_b32 s60, s60, 14\n"
        "s_and_b32 s92, s59, 7\n"
        "s_lshl_b32 s92, s92, 10\n"
        "s_add_u32 s60, s60, s92\n"
        "s_add_u32 s60, s60, 2097152\n"
        "s_and_b32 s92, s82, 7\n"
        "s_lshl_b32 s92, s92, 1\n"
        "s_add_u32 s92, s92, s83\n"
        "s_lshl_b32 s92, s92, 1\n"
        "s_add_u32 s92, s92, s88\n"
        "s_lshl_b32 s93, s59, 10\n"
        "s_cmp_lt_u32 s82, 8\n"
        "s_cselect_b32 s61, s93, s60\n"
        "s_cselect_b32 s93, 16, 17\n"
        "s_cselect_b32 s65, 0x3eb8aa3b, 1.0\n"
        "s_lshl_b32 s92, s92, s93\n"
        "s_add_u32 s61, s61, s92\n"
        "s_xor_b32 s62, s61, 0x400\n"
        "s_mul_i32 s92, s59, 56\n"
        "s_add_u32 s92, s92, s83\n"
        "s_lshl_b32 s92, s92, 11\n"
        "s_add_u32 s92, s92, 8388608\n"
        "s_lshl_b32 s93, s82, 12\n"
        "s_add_u32 s64, s92, s93\n"
        "s_lshl_b32 s93, s63, 12\n"
        "s_add_u32 s92, s92, s93\n"
        "s_sub_u32 s93, s63, 16\n"
        "s_lshl_b32 s93, s93, 1\n"
        "s_add_u32 s93, s93, s83\n"
        "s_lshl_b32 s93, s93, 18\n"
        "s_add_u32 s93, s93, s60\n"
        "s_add_u32 s93, s93, 0x2000\n"
        "s_cmp_eq_u32 s79, 3\n"
        "s_cselect_b32 s63, s93, s92\n"
        "s_cselect_b32 s92, 17, 10\n"
        "s_cselect_b32 s93, 1, 0x70\n"
        "s_lshl_b32 s92, s88, s92\n"
        "s_add_u32 s63, s63, s92\n"
        "s_lshl_b32 s93, s93, 10\n"
        "s_lshl_b32 s92, s88, 1\n"
        "s_sub_u32 s92, 1, s92\n"
        "s_mul_i32 s93, s93, s92\n"
        "s_add_u32 s51, s63, s93\n"
        "s_lshl_b32 s92, s82, 4\n"
        "s_add_u32 s92, s92, s81\n"
        "s_lshl_b32 s92, s92, 7\n"
        "s_add_u32 s66, s92, 16842752\n"
        "s_lshl_b32 s92, s81, 4\n"
        "s_add_u32 s92, s92, s82\n"
        "s_lshl_b32 s67, s92, 7\n"
        "s_add_u32 s67, s67, 16908288\n"
        "s_lshl_b32 s92, s92, 3\n"
        "s_add_u32 s92, s92, s52\n"
        "s_lshl_b32 s71, s92, 10\n"
        "s_lshl_b32 s92, s92, 7\n"
        "s_add_u32 s72, s92, 17039360\n"
        "s_lshl_b32 s92, s80, 5\n"
        "s_add_u32 s92, s92, s85\n"
        "s_lshl_b32 s92, s92, 7\n"
        "s_add_u32 s68, s92, 16973824\n"
        "s_lshl_b32 s92, s80, 2\n"
        "s_add_u32 s92, s92, s84\n"
        "s_lshl_b32 s92, s92, 1\n"
        "s_add_u32 s92, s92, s83\n"
        "s_lshl_b32 s93, s85, 6\n"
        "s_add_u32 s93, s93, s92\n"
        "s_lshl_b32 s69, s93, 10\n"
        "s_xor_b32 s70, s69, 0x400\n"
        "s_lshl_b32 s92, s92, 5\n"
        "s_add_u32 s92, s92, s85\n"
        "s_lshl_b32 s92, s92, 10\n"
        "s_add_u32 s73, s92, 6291456\n"
        "s_lshl_b32 s76, s85, 17\n"
        "s_add_u32 s76, s76, 2097152\n"
        "s_lshl_b32 s92, s86, 1\n"
        "s_add_u32 s92, s92, s88\n"
        "s_lshl_b32 s92, s92, 2\n"
        "s_add_u32 s92, s92, s89\n"
        "s_mul_i32 s74, s92, 28672\n"
        "s_add_u32 s74, s74, 8388608\n"
        "s_lshl_b32 s75, s92, 13\n"
        "s_add_u32 s75, s75, 6291456\n"
        "s_lshl_b32 s92, s88, 14\n"
        "s_lshl_b32 s93, s87, 7\n"
        "s_add_u32 s92, s92, s93\n"
        "s_mul_i32 s90, s89, 458752\n"
        "s_add_u32 s90, s90, s92\n"
        "s_lshl_b32 s91, s89, 17\n"
        "s_add_u32 s91, s91, s92\n"
        "s_and_b32 s94, s85, 7\n"
        "s_lshl_b32 s94, s94, 14\n"
        "s_mov_b32 s0, 0xffff\n"
        "s_mov_b32 s1, 0xffff\n"
        "s_mov_b32 s50, 0xbdd2d3e7\n"
        "v_lshrrev_b32 v61, 4, v124\n"
        "s_add_u32 s92, s76, s54\n"
        "v_add_u32 v125, s92, v124\n"
        "s_lshr_b32 s59, s84, 1\n"
        "s_lshl_b32 s92, s78, 4\n"
        "s_lshl_b32 s93, s80, 1\n"
        "s_add_u32 s93, s92, s93\n"
        "s_add_u32 s93, s93, s59\n"
        "v_mov_b32 v50, s93\n"
        "s_add_u32 s92, s92, 0x80\n"
        "s_and_b32 s93, s78, 6\n"
        "s_lshl_b32 s93, s93, 5\n"
        "s_add_u32 s93, s93, 48\n"
        "s_cmp_eq_u32 s83, 0\n"
        "s_cselect_b32 s92, s92, s93\n"
        "s_add_u32 s92, s92, s59\n"
        "v_lshrrev_b32 v51, 2, v61\n"
        "s_mov_b32 exec_hi, 0\n"
        "v_lshl_add_u32 v50, v51, 1, s92\n"
        "s_mov_b64 exec, -1\n"
        "v_lshlrev_b32 v50, 7, v50\n"
        "v_add_u32 v123, 16842752, v50\n"
        "v_lshrrev_b32 v52, 4, v124\n"
        "v_mov_b32 v96, 0xdddd\n"
        "s_lshl_b32 s59, s83, 10\n"
        "v_add_u32 v97, s59, v124\n"
        "v_and_b32 v98, 0xf0, v124\n"
        "v_add_u32 v98, 156672, v98\n"
        "v_lshrrev_b32 v53, 1, v124\n"
        "v_and_b32 v53, 0x100, v53\n"
        "s_sub_u32 s59, s59, 148480\n"
        "s_mov_b64 exec, s[0:1]\n"
        "v_add3_u32 v98, v98, v53, s59\n"
        "s_mov_b64 exec, -1\n"
        "v_and_b32 v99, 0x1c0, v124\n"
        "v_lshlrev_b32 v99, 1, v99\n"
        "v_and_b32 v53, 0x200, v124\n"
        "v_lshl_or_b32 v99, v53, 2, v99\n"
        "v_and_b32 v53, 24, v52\n"
        "v_lshl_or_b32 v99, v53, 2, v99\n"
        "v_and_b32 v53, 3, v52\n"
        "v_lshl_or_b32 v99, v53, 3, v99\n"
        "s_lshl_b32 s59, s89, 12\n"
        "s_add_u32 s59, s59, 0x10000\n"
        "v_add_u32 v99, s59, v99\n"
        "v_and_b32 v100, 0xf0, v124\n"
        "v_and_b32 v53, 0x300, v124\n"
        "v_lshl_or_b32 v100, v53, 1, v100\n"
        "v_lshrrev_b32 v54, 3, v52\n"
        "v_and_b32 v53, 6, v54\n"
        "v_and_b32 v101, 7, v52\n"
        "v_xor_b32 v53, v53, v101\n"
        "v_lshlrev_b32 v53, 4, v53\n"
        "v_lshl_add_u32 v101, v54, 10, v53\n"
        "v_and_b32 v54, 4, v54\n"
        "v_lshl_add_u32 v101, v54, 10, v101\n"
        "s_lshr_b32 s59, s86, 1\n"
        "s_lshl_b32 s59, s59, 11\n"
        "s_add_u32 s59, s59, 16908288\n"
        "v_lshrrev_b32 v53, 2, v52\n"
        "v_lshl_add_u32 v102, v53, 7, s59\n"
        "s_lshl_b32 s59, s86, 1\n"
        "s_add_u32 s59, s59, s88\n"
        "s_lshl_b32 s59, s59, 12\n"
        "s_add_u32 s59, s59, 17039360\n"
        "v_and_b32 v54, 31, v52\n"
        "v_lshl_add_u32 v103, v54, 7, s59\n"
        "v_mov_b32 v56, 0\n"
        "s_lshl_b32 s59, s84, 10\n"
        "s_add_u32 s59, s59, 156672\n"
        "v_add_u32 v60, s59, v124\n"
        "s_mov_b32 exec_lo, 0x110011\n"
        "s_mov_b32 exec_hi, 0x110011\n"
        "v_mov_b32 v56, 0x3f803f80\n"
        "s_mov_b64 exec, -1\n"
        "v_mov_b32 v57, v56\n"
        "v_mov_b32 v58, v56\n"
        "v_mov_b32 v59, v56\n"
        "ds_write_b128 v60, v[56:59]\n"
        "s_add_u32 m0, s53, 16384\n"
        "s_xor_b32 s57, s3, 1\n"
        "s_lshl_b32 s58, s57, 7\n"
        "buffer_load_dwordx4 v104, s[4:7], s58 offen lds\n"
        "s_add_u32 m0, s53, 24576\n"
        "s_add_u32 s58, s58, 0x10000\n"
        "buffer_load_dwordx4 v104, s[4:7], s58 offen lds\n"
        "s_add_u32 m0, s53, 57344\n"
        "s_mul_i32 s58, s57, 196608\n"
        "buffer_load_dwordx4 v105, s[8:11], s58 offen lds\n"
        "s_add_u32 m0, s53, 90112\n"
        "s_mul_i32 s58, s57, s56\n"
        "buffer_load_dwordx4 v106, s[40:43], s58 offen lds\n"
        "s_add_u32 m0, s53, 98304\n"
        "s_nop 0\n"
        "buffer_load_dwordx4 v107, s[44:47], s58 offen lds\n"
        "s_cmp_eq_u32 s79, 3\n"
        "s_cbranch_scc0 LTB%=\n"
        "s_branch LPAD%=\n"
        ".fill 120, 4, 0xbf800000\n"
        "LPAD%=:\n"
        ".p2align 3\n"
        "s_waitcnt vmcnt(5) lgkmcnt(0)\n"
        "s_barrier\n"
        "ds_read_b128 v[128:131], v110 offset:0\n"
        "ds_read_b64_tr_b16 v[80:81], v48 offset:0\n"
        "ds_read_b128 v[132:135], v110 offset:2048\n"
        "ds_read_b64_tr_b16 v[82:83], v48 offset:512\n"
        "ds_read_b128 v[64:67], v62 offset:0\n"
        "ds_read_b128 v[68:71], v62 offset:2048\n"
        "ds_read_b64_tr_b16 v[84:85], v48 offset:24576\n"
        "ds_read_b64_tr_b16 v[86:87], v48 offset:25088\n"
        "s_waitcnt lgkmcnt(4)\n"
        "v_mfma_f32_16x16x32_bf16 v[0:3], v[80:83], v[128:131], 0\n"
        "ds_read_b64_tr_b16 v[88:89], v48 offset:32768\n"
        "v_mfma_f32_16x16x32_bf16 v[4:7], v[80:83], v[132:135], 0\n"
        "ds_read_b64_tr_b16 v[90:91], v48 offset:33280\n"
        "s_add_u32 m0, s53, 32768\n"
        "s_xor_b32 s57, s3, 2\n"
        "s_lshl_b32 s58, s57, 7\n"
        "buffer_load_dwordx4 v104, s[4:7], s58 offen lds\n"
        "s_setprio 3\n"
        "s_waitcnt lgkmcnt(5)\n"
        "v_mfma_f32_16x16x32_bf16 v[8:11], v[80:83], v[64:67], 0\n"
        "s_waitcnt lgkmcnt(4)\n"
        "v_mfma_f32_16x16x32_bf16 v[12:15], v[80:83], v[68:71], 0\n"
        "s_add_u32 m0, s53, 40960\n"
        "s_add_u32 s58, s58, 0x10000\n"
        "buffer_load_dwordx4 v104, s[4:7], s58 offen lds\n"
        "s_waitcnt lgkmcnt(2)\n"
        "v_mfma_f32_16x16x32_bf16 v[16:19], v[128:131], v[84:87], 0\n"
        "ds_read_b128 v[136:139], v111 offset:0\n"
        "ds_read_b64_tr_b16 v[92:93], v48 offset:4096\n"
        "v_mfma_f32_16x16x32_bf16 v[20:23], v[132:135], v[84:87], 0\n"
        "ds_read_b128 v[140:143], v111 offset:2048\n"
        "ds_read_b64_tr_b16 v[94:95], v48 offset:4608\n"
        "s_add_u32 m0, s53, 65536\n"
        "s_mul_i32 s58, s57, 196608\n"
        "buffer_load_dwordx4 v105, s[8:11], s58 offen lds\n"
        "v_mfma_f32_16x16x32_bf16 v[24:27], v[64:67], v[84:87], 0\n"
        "ds_read_b128 v[72:75], v63 offset:0\n"
        "v_mfma_f32_16x16x32_bf16 v[28:31], v[68:71], v[84:87], 0\n"
        "ds_read_b128 v[76:79], v63 offset:2048\n"
        "s_add_u32 m0, s53, 106496\n"
        "s_mul_i32 s58, s57, s56\n"
        "buffer_load_dwordx4 v106, s[40:43], s58 offen lds\n"
        "s_waitcnt lgkmcnt(6)\n"
        "v_mfma_f32_16x16x32_bf16 v[32:35], v[128:131], v[88:91], 0\n"
        "ds_read_b64_tr_b16 v[112:113], v48 offset:28672\n"
        "v_mfma_f32_16x16x32_bf16 v[36:39], v[132:135], v[88:91], 0\n"
        "ds_read_b64_tr_b16 v[114:115], v48 offset:29184\n"
        "s_setprio 2\n"
        "v_mfma_f32_16x16x32_bf16 v[40:43], v[64:67], v[88:91], 0\n"
        "v_mfma_f32_16x16x32_bf16 v[44:47], v[68:71], v[88:91], 0\n"
        "s_add_u32 m0, s53, 114688\n"
        "s_nop 0\n"
        "buffer_load_dwordx4 v107, s[44:47], s58 offen lds\n"
        "s_waitcnt lgkmcnt(4)\n"
        "v_mfma_f32_16x16x32_bf16 v[0:3], v[92:95], v[136:139], v[0:3]\n"
        "ds_read_b64_tr_b16 v[80:81], v48 offset:36864\n"
        "v_mfma_f32_16x16x32_bf16 v[4:7], v[92:95], v[140:143], v[4:7]\n"
        "ds_read_b64_tr_b16 v[82:83], v48 offset:37376\n"
        "s_setprio 1\n"
        "s_waitcnt lgkmcnt(4)\n"
        "v_mfma_f32_16x16x32_bf16 v[8:11], v[92:95], v[72:75], v[8:11]\n"
        "v_mfma_f32_16x16x32_bf16 v[12:15], v[92:95], v[76:79], v[12:15]\n"
        "s_setprio 0\n"
        "s_waitcnt lgkmcnt(2)\n"
        "v_mfma_f32_16x16x32_bf16 v[16:19], v[136:139], v[112:115], v[16:19]\n"
        "s_waitcnt vmcnt(5) lgkmcnt(0)\n"
        "s_barrier\n"
        "ds_read_b128 v[160:163], v110 offset:16384\n"
        "ds_read_b64_tr_b16 v[84:85], v48 offset:8192\n"
        "v_mfma_f32_16x16x32_bf16 v[20:23], v[140:143], v[112:115], v[20:23]\n"
        "ds_read_b128 v[164:167], v110 offset:18432\n"
        "ds_read_b64_tr_b16 v[86:87], v48 offset:8704\n"
        "s_setprio 3\n"
        "v_mfma_f32_16x16x32_bf16 v[24:27], v[72:75], v[112:115], v[24:27]\n"
        "ds_read_b128 v[64:67], v62 offset:16384\n"
        "v_mfma_f32_16x16x32_bf16 v[28:31], v[76:79], v[112:115], v[28:31]\n"
        "ds_read_b128 v[68:71], v62 offset:18432\n"
        "s_mov_b32 m0, s53\n"
        "s_xor_b32 s57, s3, 3\n"
        "s_lshl_b32 s58, s57, 7\n"
        "buffer_load_dwordx4 v104, s[4:7], s58 offen lds\n"
        "v_mfma_f32_16x16x32_bf16 v[32:35], v[136:139], v[80:83], v[32:35]\n"
        "ds_read_b64_tr_b16 v[88:89], v48 offset:40960\n"
        "v_mfma_f32_16x16x32_bf16 v[36:39], v[140:143], v[80:83], v[36:39]\n"
        "ds_read_b64_tr_b16 v[90:91], v48 offset:41472\n"
        "s_mul_i32 s92, s48, 0x5851f42d\n"
        "s_mul_hi_u32 s93, s48, 0x4c957f2d\n"
        "s_add_u32 s92, s92, s93\n"
        "v_mfma_f32_16x16x32_bf16 v[40:43], v[72:75], v[80:83], v[40:43]\n"
        "s_mul_i32 s49, s49, 0x4c957f2d\n"
        "s_add_u32 s49, s49, s92\n"
        "s_mul_i32 s48, s48, 0x4c957f2d\n"
        "v_mfma_f32_16x16x32_bf16 v[44:47], v[76:79], v[80:83], v[44:47]\n"
        "s_add_u32 m0, s53, 8192\n"
        "s_add_u32 s58, s58, 0x10000\n"
        "buffer_load_dwordx4 v104, s[4:7], s58 offen lds\n"
        "s_add_u32 s48, s48, 0xf767814f\n"
        "s_addc_u32 s49, s49, 0x14057b7e\n"
        "s_waitcnt lgkmcnt(4)\n"
        "v_mfma_f32_16x16x32_bf16 v[0:3], v[84:87], v[160:163], v[0:3]\n"
        "ds_read_b64_tr_b16 v[92:93], v49 offset:0\n"
        "v_mfma_f32_16x16x32_bf16 v[4:7], v[84:87], v[164:167], v[4:7]\n"
        "ds_read_b64_tr_b16 v[94:95], v49 offset:512\n"
        "s_nop 5\n"
        "s_waitcnt lgkmcnt(4)\n"
        "v_mfma_f32_16x16x32_bf16 v[8:11], v[84:87], v[64:67], v[8:11]\n"
        "v_mfma_f32_16x16x32_bf16 v[12:15], v[84:87], v[68:71], v[12:15]\n"
        "s_nop 5\n"
        "s_add_u32 m0, s53, 49152\n"
        "s_mul_i32 s58, s57, 196608\n"
        "buffer_load_dwordx4 v105, s[8:11], s58 offen lds\n"
        "s_waitcnt lgkmcnt(2)\n"
        "v_mfma_f32_16x16x32_bf16 v[16:19], v[160:163], v[88:91], v[16:19]\n"
        "ds_read_b128 v[168:171], v111 offset:16384\n"
        "ds_read_b64_tr_b16 v[112:113], v48 offset:12288\n"
        "v_mfma_f32_16x16x32_bf16 v[20:23], v[164:167], v[88:91], v[20:23]\n"
        "ds_read_b128 v[172:175], v111 offset:18432\n"
        "ds_read_b64_tr_b16 v[114:115], v48 offset:12800\n"
        "s_nop 5\n"
        "s_setprio 2\n"
        "v_mfma_f32_16x16x32_bf16 v[24:27], v[64:67], v[88:91], v[24:27]\n"
        "ds_read_b128 v[72:75], v63 offset:16384\n"
        "v_mfma_f32_16x16x32_bf16 v[28:31], v[68:71], v[88:91], v[28:31]\n"
        "ds_read_b128 v[76:79], v63 offset:18432\n"
        "s_nop 5\n"
        "s_add_u32 m0, s53, 122880\n"
        "s_mul_i32 s58, s57, s56\n"
        "buffer_load_dwordx4 v106, s[40:43], s58 offen lds\n"
        "s_waitcnt lgkmcnt(6)\n"
        "v_mfma_f32_16x16x32_bf16 v[32:35], v[160:163], v[92:95], v[32:35]\n"
        "ds_read_b64_tr_b16 v[80:81], v48 offset:45056\n"
        "v_mfma_f32_16x16x32_bf16 v[36:39], v[164:167], v[92:95], v[36:39]\n"
        "ds_read_b64_tr_b16 v[82:83], v48 offset:45568\n"
        "s_nop 5\n"
        "v_mfma_f32_16x16x32_bf16 v[40:43], v[64:67], v[92:95], v[40:43]\n"
        "v_mfma_f32_16x16x32_bf16 v[44:47], v[68:71], v[92:95], v[44:47]\n"
        "s_nop 5\n"
        "s_add_u32 m0, s53, 131072\n"
        "s_nop 0\n"
        "buffer_load_dwordx4 v107, s[44:47], s58 offen lds\n"
        "s_setprio 1\n"
        "s_waitcnt lgkmcnt(4)\n"
        "v_mfma_f32_16x16x32_bf16 v[0:3], v[112:115], v[168:171], v[0:3]\n"
        "ds_read_b64_tr_b16 v[84:85], v49 offset:4096\n"
        "v_mfma_f32_16x16x32_bf16 v[4:7], v[112:115], v[172:175], v[4:7]\n"
        "ds_read_b64_tr_b16 v[86:87], v49 offset:4608\n"
        "s_nop 5\n"
        "s_waitcnt lgkmcnt(4)\n"
        "v_mfma_f32_16x16x32_bf16 v[8:11], v[112:115], v[72:75], v[8:11]\n"
        "v_mfma_f32_16x16x32_bf16 v[12:15], v[112:115], v[76:79], v[12:15]\n"
        "s_nop 5\n"
        "s_setprio 0\n"
        "s_waitcnt lgkmcnt(2)\n"
        "v_mfma_f32_16x16x32_bf16 v[16:19], v[168:171], v[80:83], v[16:19]\n"
        "s_waitcnt vmcnt(5) lgkmcnt(0)\n"
        "s_barrier\n"
        "ds_read_b128 v[144:147], v110 offset:32768\n"
        "ds_read_b64_tr_b16 v[88:89], v48 offset:16384\n"
        "v_mfma_f32_16x16x32_bf16 v[20:23], v[172:175], v[80:83], v[20:23]\n"
        "ds_read_b128 v[148:151], v110 offset:34816\n"
        "ds_read_b64_tr_b16 v[90:91], v48 offset:16896\n"
        "s_nop 5\n"
        "s_setprio 3\n"
        "v_mfma_f32_16x16x32_bf16 v[24:27], v[72:75], v[80:83], v[24:27]\n"
        "ds_read_b128 v[64:67], v62 offset:32768\n"
        "v_mfma_f32_16x16x32_bf16 v[28:31], v[76:79], v[80:83], v[28:31]\n"
        "ds_read_b128 v[68:71], v62 offset:34816\n"
        "s_nop 5\n"
        "s_add_u32 m0, s53, 16384\n"
        "s_xor_b32 s57, s3, 4\n"
        "s_lshl_b32 s58, s57, 7\n"
        "buffer_load_dwordx4 v104, s[4:7], s58 offen lds\n"
        "v_mfma_f32_16x16x32_bf16 v[32:35], v[168:171], v[84:87], v[32:35]\n"
        "ds_read_b64_tr_b16 v[92:93], v49 offset:8192\n"
        "v_mfma_f32_16x16x32_bf16 v[36:39], v[172:175], v[84:87], v[36:39]\n"
        "ds_read_b64_tr_b16 v[94:95], v49 offset:8704\n"
        "s_nop 5\n"
        "v_mfma_f32_16x16x32_bf16 v[40:43], v[72:75], v[84:87], v[40:43]\n"
        "v_mfma_f32_16x16x32_bf16 v[44:47], v[76:79], v[84:87], v[44:47]\n"
        "s_nop 5\n"
        "s_add_u32 m0, s53, 24576\n"
        "s_add_u32 s58, s58, 0x10000\n"
        "buffer_load_dwordx4 v104, s[4:7], s58 offen lds\n"
        "s_waitcnt lgkmcnt(4)\n"
        "v_mfma_f32_16x16x32_bf16 v[0:3], v[88:91], v[144:147], v[0:3]\n"
        "ds_read_b64_tr_b16 v[112:113], v49 offset:16384\n"
        "v_mfma_f32_16x16x32_bf16 v[4:7], v[88:91], v[148:151], v[4:7]\n"
        "ds_read_b64_tr_b16 v[114:115], v49 offset:16896\n"
        "s_nop 5\n"
        "s_waitcnt lgkmcnt(4)\n"
        "v_mfma_f32_16x16x32_bf16 v[8:11], v[88:91], v[64:67], v[8:11]\n"
        "v_mfma_f32_16x16x32_bf16 v[12:15], v[88:91], v[68:71], v[12:15]\n"
        "s_nop 5\n"
        "s_add_u32 m0, s53, 57344\n"
        "s_mul_i32 s58, s57, 196608\n"
        "buffer_load_dwordx4 v105, s[8:11], s58 offen lds\n"
        "s_waitcnt lgkmcnt(2)\n"
        "v_mfma_f32_16x16x32_bf16 v[16:19], v[144:147], v[92:95], v[16:19]\n"
        "ds_read_b128 v[152:155], v111 offset:32768\n"
        "ds_read_b64_tr_b16 v[80:81], v48 offset:20480\n"
        "v_mfma_f32_16x16x32_bf16 v[20:23], v[148:151], v[92:95], v[20:23]\n"
        "ds_read_b128 v[156:159], v111 offset:34816\n"
        "ds_read_b64_tr_b16 v[82:83], v48 offset:20992\n"
        "s_nop 5\n"
        "s_setprio 2\n"
        "v_mfma_f32_16x16x32_bf16 v[24:27], v[64:67], v[92:95], v[24:27]\n"
        "ds_read_b128 v[72:75], v63 offset:32768\n"
        "v_mfma_f32_16x16x32_bf16 v[28:31], v[68:71], v[92:95], v[28:31]\n"
        "ds_read_b128 v[76:79], v63 offset:34816\n"
        "s_nop 5\n"
        "s_add_u32 m0, s53, 139264\n"
        "s_mul_i32 s58, s57, s56\n"
        "buffer_load_dwordx4 v106, s[40:43], s58 offen lds\n"
        "s_waitcnt lgkmcnt(6)\n"
        "v_mfma_f32_16x16x32_bf16 v[32:35], v[144:147], v[112:115], v[32:35]\n"
        "ds_read_b64_tr_b16 v[84:85], v49 offset:12288\n"
        "v_mfma_f32_16x16x32_bf16 v[36:39], v[148:151], v[112:115], v[36:39]\n"
        "ds_read_b64_tr_b16 v[86:87], v49 offset:12800\n"
        "s_nop 5\n"
        "v_mfma_f32_16x16x32_bf16 v[40:43], v[64:67], v[112:115], v[40:43]\n"
        "v_mfma_f32_16x16x32_bf16 v[44:47], v[68:71], v[112:115], v[44:47]\n"
        "s_nop 5\n"
        "s_add_u32 m0, s53, 147456\n"
        "s_nop 0\n"
        "buffer_load_dwordx4 v107, s[44:47], s58 offen lds\n"
        "s_setprio 1\n"
        "s_waitcnt lgkmcnt(4)\n"
        "v_mfma_f32_16x16x32_bf16 v[0:3], v[80:83], v[152:155], v[0:3]\n"
        "ds_read_b64_tr_b16 v[88:89], v49 offset:20480\n"
        "v_mfma_f32_16x16x32_bf16 v[4:7], v[80:83], v[156:159], v[4:7]\n"
        "ds_read_b64_tr_b16 v[90:91], v49 offset:20992\n"
        "s_nop 5\n"
        "s_waitcnt lgkmcnt(4)\n"
        "v_mfma_f32_16x16x32_bf16 v[8:11], v[80:83], v[72:75], v[8:11]\n"
        "v_mfma_f32_16x16x32_bf16 v[12:15], v[80:83], v[76:79], v[12:15]\n"
        "s_nop 5\n"
        "s_setprio 0\n"
        "s_waitcnt lgkmcnt(2)\n"
        "v_mfma_f32_16x16x32_bf16 v[16:19], v[152:155], v[84:87], v[16:19]\n"
        "s_waitcnt vmcnt(5) lgkmcnt(0)\n"
        "s_barrier\n"
        "ds_read_b128 v[192:195], v110 offset:0\n"
        "ds_read_b64_tr_b16 v[92:93], v48 offset:0\n"
        "v_mfma_f32_16x16x32_bf16 v[20:23], v[156:159], v[84:87], v[20:23]\n"
        "ds_read_b128 v[196:199], v110 offset:2048\n"
        "ds_read_b64_tr_b16 v[94:95], v48 offset:512\n"
        "s_nop 5\n"
        "s_setprio 3\n"
        "v_mfma_f32_16x16x32_bf16 v[24:27], v[72:75], v[84:87], v[24:27]\n"
        "ds_read_b128 v[64:67], v62 offset:0\n"
        "v_mfma_f32_16x16x32_bf16 v[28:31], v[76:79], v[84:87], v[28:31]\n"
        "ds_read_b128 v[68:71], v62 offset:2048\n"
        "s_nop 5\n"
        "s_add_u32 m0, s53, 32768\n"
        "s_xor_b32 s57, s3, 5\n"
        "s_lshl_b32 s58, s57, 7\n"
        "buffer_load_dwordx4 v104, s[4:7], s58 offen lds\n"
        "v_mfma_f32_16x16x32_bf16 v[32:35], v[152:155], v[88:91], v[32:35]\n"
        "ds_read_b64_tr_b16 v[112:113], v49 offset:24576\n"
        "v_mfma_f32_16x16x32_bf16 v[36:39], v[156:159], v[88:91], v[36:39]\n"
        "ds_read_b64_tr_b16 v[114:115], v49 offset:25088\n"
        "s_nop 5\n"
        "v_mfma_f32_16x16x32_bf16 v[40:43], v[72:75], v[88:91], v[40:43]\n"
        "v_mfma_f32_16x16x32_bf16 v[44:47], v[76:79], v[88:91], v[44:47]\n"
        "s_nop 5\n"
        "s_add_u32 m0, s53, 40960\n"
        "s_add_u32 s58, s58, 0x10000\n"
        "buffer_load_dwordx4 v104, s[4:7], s58 offen lds\n"
        "s_waitcnt lgkmcnt(4)\n"
        "v_mfma_f32_16x16x32_bf16 v[0:3], v[92:95], v[192:195], v[0:3]\n"
        "ds_read_b64_tr_b16 v[80:81], v49 offset:32768\n"
        "v_mfma_f32_16x16x32_bf16 v[4:7], v[92:95], v[196:199], v[4:7]\n"
        "ds_read_b64_tr_b16 v[82:83], v49 offset:33280\n"
        "s_waitcnt lgkmcnt(4)\n"
        "v_mfma_f32_16x16x32_bf16 v[8:11], v[92:95], v[64:67], v[8:11]\n"
        "v_mfma_f32_16x16x32_bf16 v[12:15], v[92:95], v[68:71], v[12:15]\n"
        "s_add_u32 m0, s53, 65536\n"
        "s_mul_i32 s58, s57, 196608\n"
        "buffer_load_dwordx4 v105, s[8:11], s58 offen lds\n"
        "s_waitcnt lgkmcnt(2)\n"
        "v_mfma_f32_16x16x32_bf16 v[16:19], v[192:195], v[112:115], v[16:19]\n"
        "ds_read_b128 v[200:203], v111 offset:0\n"
        "ds_read_b64_tr_b16 v[84:85], v48 offset:4096\n"
        "v_mfma_f32_16x16x32_bf16 v[20:23], v[196:199], v[112:115], v[20:23]\n"
        "ds_read_b128 v[204:207], v111 offset:2048\n"
        "ds_read_b64_tr_b16 v[86:87], v48 offset:4608\n"
        "s_setprio 2\n"
        "v_mfma_f32_16x16x32_bf16 v[24:27], v[64:67], v[112:115], v[24:27]\n"
        "ds_read_b128 v[72:75], v63 offset:0\n"
        "v_mfma_f32_16x16x32_bf16 v[28:31], v[68:71], v[112:115], v[28:31]\n"
        "ds_read_b128 v[76:79], v63 offset:2048\n"
        "s_add_u32 m0, s53, 73728\n"
        "s_mul_i32 s58, s57, s56\n"
        "buffer_load_dwordx4 v106, s[40:43], s58 offen lds\n"
        "s_waitcnt lgkmcnt(6)\n"
        "v_mfma_f32_16x16x32_bf16 v[32:35], v[192:195], v[80:83], v[32:35]\n"
        "ds_read_b64_tr_b16 v[88:89], v49 offset:28672\n"
        "v_mfma_f32_16x16x32_bf16 v[36:39], v[196:199], v[80:83], v[36:39]\n"
        "ds_read_b64_tr_b16 v[90:91], v49 offset:29184\n"
        "v_mfma_f32_16x16x32_bf16 v[40:43], v[64:67], v[80:83], v[40:43]\n"
        "v_mfma_f32_16x16x32_bf16 v[44:47], v[68:71], v[80:83], v[44:47]\n"
        "s_add_u32 m0, s53, 81920\n"
        "s_nop 0\n"
        "buffer_load_dwordx4 v107, s[44:47], s58 offen lds\n"
        "s_setprio 1\n"
        "s_waitcnt lgkmcnt(4)\n"
        "v_mfma_f32_16x16x32_bf16 v[0:3], v[84:87], v[200:203], v[0:3]\n"
        "ds_read_b64_tr_b16 v[92:93], v49 offset:36864\n"
        "v_mfma_f32_16x16x32_bf16 v[4:7], v[84:87], v[204:207], v[4:7]\n"
        "ds_read_b64_tr_b16 v[94:95], v49 offset:37376\n"
        "s_waitcnt lgkmcnt(4)\n"
        "v_mfma_f32_16x16x32_bf16 v[8:11], v[84:87], v[72:75], v[8:11]\n"
        "v_mfma_f32_16x16x32_bf16 v[12:15], v[84:87], v[76:79], v[12:15]\n"
        "s_setprio 0\n"
        "s_waitcnt lgkmcnt(2)\n"
        "v_mfma_f32_16x16x32_bf16 v[16:19], v[200:203], v[88:91], v[16:19]\n"
        "s_waitcnt vmcnt(5) lgkmcnt(0)\n"
        "s_barrier\n"
        "ds_read_b128 v[176:179], v110 offset:16384\n"
        "ds_read_b64_tr_b16 v[112:113], v48 offset:8192\n"
        "v_mfma_f32_16x16x32_bf16 v[20:23], v[204:207], v[88:91], v[20:23]\n"
        "ds_read_b128 v[180:183], v110 offset:18432\n"
        "ds_read_b64_tr_b16 v[114:115], v48 offset:8704\n"
        "s_setprio 3\n"
        "v_mfma_f32_16x16x32_bf16 v[24:27], v[72:75], v[88:91], v[24:27]\n"
        "ds_read_b128 v[64:67], v62 offset:16384\n"
        "v_mfma_f32_16x16x32_bf16 v[28:31], v[76:79], v[88:91], v[28:31]\n"
        "ds_read_b128 v[68:71], v62 offset:18432\n"
        "s_mov_b32 m0, s53\n"
        "s_xor_b32 s57, s3, 6\n"
        "s_lshl_b32 s58, s57, 7\n"
        "buffer_load_dwordx4 v104, s[4:7], s58 offen lds\n"
        "v_mfma_f32_16x16x32_bf16 v[32:35], v[200:203], v[92:95], v[32:35]\n"
        "ds_read_b64_tr_b16 v[80:81], v49 offset:40960\n"
        "v_mfma_f32_16x16x32_bf16 v[36:39], v[204:207], v[92:95], v[36:39]\n"
        "ds_read_b64_tr_b16 v[82:83], v49 offset:41472\n"
        "v_mfma_f32_16x16x32_bf16 v[40:43], v[72:75], v[92:95], v[40:43]\n"
        "v_mfma_f32_16x16x32_bf16 v[44:47], v[76:79], v[92:95], v[44:47]\n"
        "s_add_u32 m0, s53, 8192\n"
        "s_add_u32 s58, s58, 0x10000\n"
        "buffer_load_dwordx4 v104, s[4:7], s58 offen lds\n"
        "s_waitcnt lgkmcnt(4)\n"
        "v_mfma_f32_16x16x32_bf16 v[0:3], v[112:115], v[176:179], v[0:3]\n"
        "ds_read_b64_tr_b16 v[84:85], v49 offset:49152\n"
        "v_mfma_f32_16x16x32_bf16 v[4:7], v[112:115], v[180:183], v[4:7]\n"
        "ds_read_b64_tr_b16 v[86:87], v49 offset:49664\n"
        "s_waitcnt lgkmcnt(4)\n"
        "v_mfma_f32_16x16x32_bf16 v[8:11], v[112:115], v[64:67], v[8:11]\n"
        "v_mfma_f32_16x16x32_bf16 v[12:15], v[112:115], v[68:71], v[12:15]\n"
        "s_add_u32 m0, s53, 49152\n"
        "s_mul_i32 s58, s57, 196608\n"
        "buffer_load_dwordx4 v105, s[8:11], s58 offen lds\n"
        "s_waitcnt lgkmcnt(2)\n"
        "v_mfma_f32_16x16x32_bf16 v[16:19], v[176:179], v[80:83], v[16:19]\n"
        "ds_read_b128 v[184:187], v111 offset:16384\n"
        "ds_read_b64_tr_b16 v[88:89], v48 offset:12288\n"
        "v_mfma_f32_16x16x32_bf16 v[20:23], v[180:183], v[80:83], v[20:23]\n"
        "ds_read_b128 v[188:191], v111 offset:18432\n"
        "ds_read_b64_tr_b16 v[90:91], v48 offset:12800\n"
        "s_setprio 2\n"
        "v_mfma_f32_16x16x32_bf16 v[24:27], v[64:67], v[80:83], v[24:27]\n"
        "ds_read_b128 v[72:75], v63 offset:16384\n"
        "v_mfma_f32_16x16x32_bf16 v[28:31], v[68:71], v[80:83], v[28:31]\n"
        "ds_read_b128 v[76:79], v63 offset:18432\n"
        "s_add_u32 m0, s53, 90112\n"
        "s_mul_i32 s58, s57, s56\n"
        "buffer_load_dwordx4 v106, s[40:43], s58 offen lds\n"
        "s_waitcnt lgkmcnt(6)\n"
        "v_mfma_f32_16x16x32_bf16 v[32:35], v[176:179], v[84:87], v[32:35]\n"
        "ds_read_b64_tr_b16 v[92:93], v49 offset:45056\n"
        "v_mfma_f32_16x16x32_bf16 v[36:39], v[180:183], v[84:87], v[36:39]\n"
        "ds_read_b64_tr_b16 v[94:95], v49 offset:45568\n"
        "v_mfma_f32_16x16x32_bf16 v[40:43], v[64:67], v[84:87], v[40:43]\n"
        "v_mfma_f32_16x16x32_bf16 v[44:47], v[68:71], v[84:87], v[44:47]\n"
        "s_add_u32 m0, s53, 98304\n"
        "s_nop 0\n"
        "buffer_load_dwordx4 v107, s[44:47], s58 offen lds\n"
        "s_setprio 1\n"
        "s_waitcnt lgkmcnt(4)\n"
        "v_mfma_f32_16x16x32_bf16 v[0:3], v[88:91], v[184:187], v[0:3]\n"
        "ds_read_b64_tr_b16 v[112:113], v49 offset:53248\n"
        "v_mfma_f32_16x16x32_bf16 v[4:7], v[88:91], v[188:191], v[4:7]\n"
        "ds_read_b64_tr_b16 v[114:115], v49 offset:53760\n"
        "s_waitcnt lgkmcnt(4)\n"
        "v_mfma_f32_16x16x32_bf16 v[8:11], v[88:91], v[72:75], v[8:11]\n"
        "v_mfma_f32_16x16x32_bf16 v[12:15], v[88:91], v[76:79], v[12:15]\n"
        "s_setprio 0\n"
        "s_waitcnt lgkmcnt(2)\n"
        "v_mfma_f32_16x16x32_bf16 v[16:19], v[184:187], v[92:95], v[16:19]\n"
        "s_waitcnt vmcnt(5) lgkmcnt(0)\n"
        "s_barrier\n"
        "ds_read_b128 v[224:227], v110 offset:32768\n"
        "ds_read_b64_tr_b16 v[80:81], v48 offset:16384\n"
        "v_mfma_f32_16x16x32_bf16 v[20:23], v[188:191], v[92:95], v[20:23]\n"
        "ds_read_b128 v[228:231], v110 offset:34816\n"
        "ds_read_b64_tr_b16 v[82:83], v48 offset:16896\n"
        "s_setprio 3\n"
        "v_mfma_f32_16x16x32_bf16 v[24:27], v[72:75], v[92:95], v[24:27]\n"
        "ds_read_b128 v[64:67], v62 offset:32768\n"
        "v_mfma_f32_16x16x32_bf16 v[28:31], v[76:79], v[92:95], v[28:31]\n"
        "ds_read_b128 v[68:71], v62 offset:34816\n"
        "s_add_u32 m0, s53, 16384\n"
        "s_xor_b32 s57, s3, 7\n"
        "s_lshl_b32 s58, s57, 7\n"
        "buffer_load_dwordx4 v104, s[4:7], s58 offen lds\n"
        "v_mfma_f32_16x16x32_bf16 v[32:35], v[184:187], v[112:115], v[32:35]\n"
        "ds_read_b64_tr_b16 v[84:85], v48 offset:24576\n"
        "v_mfma_f32_16x16x32_bf16 v[36:39], v[188:191], v[112:115], v[36:39]\n"
        "ds_read_b64_tr_b16 v[86:87], v48 offset:25088\n"
        "v_mfma_f32_16x16x32_bf16 v[40:43], v[72:75], v[112:115], v[40:43]\n"
        "v_mfma_f32_16x16x32_bf16 v[44:47], v[76:79], v[112:115], v[44:47]\n"
        "s_add_u32 m0, s53, 24576\n"
        "s_add_u32 s58, s58, 0x10000\n"
        "buffer_load_dwordx4 v104, s[4:7], s58 offen lds\n"
        "s_waitcnt lgkmcnt(4)\n"
        "v_mfma_f32_16x16x32_bf16 v[0:3], v[80:83], v[224:227], v[0:3]\n"
        "ds_read_b64_tr_b16 v[88:89], v48 offset:32768\n"
        "v_mfma_f32_16x16x32_bf16 v[4:7], v[80:83], v[228:231], v[4:7]\n"
        "ds_read_b64_tr_b16 v[90:91], v48 offset:33280\n"
        "s_waitcnt lgkmcnt(4)\n"
        "v_mfma_f32_16x16x32_bf16 v[8:11], v[80:83], v[64:67], v[8:11]\n"
        "v_mfma_f32_16x16x32_bf16 v[12:15], v[80:83], v[68:71], v[12:15]\n"
        "s_add_u32 m0, s53, 57344\n"
        "s_mul_i32 s58, s57, 196608\n"
        "buffer_load_dwordx4 v105, s[8:11], s58 offen lds\n"
        "s_waitcnt lgkmcnt(2)\n"
        "v_mfma_f32_16x16x32_bf16 v[16:19], v[224:227], v[84:87], v[16:19]\n"
        "ds_read_b128 v[232:235], v111 offset:32768\n"
        "ds_read_b64_tr_b16 v[92:93], v48 offset:20480\n"
        "v_mfma_f32_16x16x32_bf16 v[20:23], v[228:231], v[84:87], v[20:23]\n"
        "ds_read_b128 v[236:239], v111 offset:34816\n"
        "ds_read_b64_tr_b16 v[94:95], v48 offset:20992\n"
        "s_setprio 2\n"
        "v_mfma_f32_16x16x32_bf16 v[24:27], v[64:67], v[84:87], v[24:27]\n"
        "ds_read_b128 v[72:75], v63 offset:32768\n"
        "v_mfma_f32_16x16x32_bf16 v[28:31], v[68:71], v[84:87], v[28:31]\n"
        "ds_read_b128 v[76:79], v63 offset:34816\n"
        "s_add_u32 m0, s53, 106496\n"
        "s_mul_i32 s58, s57, s56\n"
        "buffer_load_dwordx4 v106, s[40:43], s58 offen lds\n"
        "s_waitcnt lgkmcnt(6)\n"
        "v_mfma_f32_16x16x32_bf16 v[32:35], v[224:227], v[88:91], v[32:35]\n"
        "ds_read_b64_tr_b16 v[112:113], v48 offset:28672\n"
        "v_mfma_f32_16x16x32_bf16 v[36:39], v[228:231], v[88:91], v[36:39]\n"
        "ds_read_b64_tr_b16 v[114:115], v48 offset:29184\n"
        "v_mfma_f32_16x16x32_bf16 v[40:43], v[64:67], v[88:91], v[40:43]\n"
        "v_mfma_f32_16x16x32_bf16 v[44:47], v[68:71], v[88:91], v[44:47]\n"
        "s_add_u32 m0, s53, 114688\n"
        "s_nop 0\n"
        "buffer_load_dwordx4 v107, s[44:47], s58 offen lds\n"
        "s_setprio 1\n"
        "s_waitcnt lgkmcnt(4)\n"
        "v_mfma_f32_16x16x32_bf16 v[0:3], v[92:95], v[232:235], v[0:3]\n"
        "ds_read_b64_tr_b16 v[80:81], v48 offset:36864\n"
        "v_mfma_f32_16x16x32_bf16 v[4:7], v[92:95], v[236:239], v[4:7]\n"
        "ds_read_b64_tr_b16 v[82:83], v48 offset:37376\n"
        "v_lshrrev_b32 v50, 7, v124\n"
        "s_lshl_b32 s59, s80, 3\n"
        "s_add_u32 s59, s59, s52\n"
        "s_mul_i32 s59, s59, 28672\n"
        "s_lshl_b32 s60, s82, 7\n"
        "s_add_u32 s59, s59, s60\n"
        "v_mul_u32_u24 v50, 0xe00, v50\n"
        "buffer_load_dword v51, v50, s[12:15], s59 offen\n"
        "buffer_load_dword v51, v50, s[16:19], s59 offen\n"
        "s_waitcnt lgkmcnt(4)\n"
        "v_mfma_f32_16x16x32_bf16 v[8:11], v[92:95], v[72:75], v[8:11]\n"
        "v_mfma_f32_16x16x32_bf16 v[12:15], v[92:95], v[76:79], v[12:15]\n"
        "s_setprio 0\n"
        "s_waitcnt lgkmcnt(2)\n"
        "v_mfma_f32_16x16x32_bf16 v[16:19], v[232:235], v[112:115], v[16:19]\n"
        "s_waitcnt vmcnt(7) lgkmcnt(0)\n"
        "s_barrier\n"
        "ds_read_b128 v[208:211], v110 offset:0\n"
        "ds_read_b64_tr_b16 v[84:85], v48 offset:0\n"
        "v_mfma_f32_16x16x32_bf16 v[20:23], v[236:239], v[112:115], v[20:23]\n"
        "ds_read_b128 v[212:215], v110 offset:2048\n"
        "ds_read_b64_tr_b16 v[86:87], v48 offset:512\n"
        "s_setprio 3\n"
        "v_mfma_f32_16x16x32_bf16 v[24:27], v[72:75], v[112:115], v[24:27]\n"
        "ds_read_b128 v[64:67], v62 offset:0\n"
        "v_mfma_f32_16x16x32_bf16 v[28:31], v[76:79], v[112:115], v[28:31]\n"
        "ds_read_b128 v[68:71], v62 offset:2048\n"
        "s_add_u32 m0, s53, 122880\n"
        "s_xor_b32 s57, s3, 0\n"
        "s_mul_i32 s58, s57, 229376\n"
        "buffer_load_dwordx4 v108, s[12:15], s58 offen lds\n"
        "v_mfma_f32_16x16x32_bf16 v[32:35], v[232:235], v[80:83], v[32:35]\n"
        "ds_read_b64_tr_b16 v[88:89], v48 offset:40960\n"
        "v_mfma_f32_16x16x32_bf16 v[36:39], v[236:239], v[80:83], v[36:39]\n"
        "ds_read_b64_tr_b16 v[90:91], v48 offset:41472\n"
        "v_mfma_f32_16x16x32_bf16 v[40:43], v[72:75], v[80:83], v[40:43]\n"
        "v_mfma_f32_16x16x32_bf16 v[44:47], v[76:79], v[80:83], v[44:47]\n"
        "s_add_u32 m0, s53, 131072\n"
        "s_nop 0\n"
        "buffer_load_dwordx4 v108, s[16:19], s58 offen lds\n"
        "s_waitcnt lgkmcnt(4)\n"
        "v_mfma_f32_16x16x32_bf16 v[0:3], v[84:87], v[208:211], v[0:3]\n"
        "ds_read_b64_tr_b16 v[92:93], v49 offset:0\n"
        "v_mfma_f32_16x16x32_bf16 v[4:7], v[84:87], v[212:215], v[4:7]\n"
        "ds_read_b64_tr_b16 v[94:95], v49 offset:512\n"
        "s_waitcnt lgkmcnt(4)\n"
        "v_mfma_f32_16x16x32_bf16 v[8:11], v[84:87], v[64:67], v[8:11]\n"
        "v_mfma_f32_16x16x32_bf16 v[12:15], v[84:87], v[68:71], v[12:15]\n"
        "s_add_u32 m0, s53, 139264\n"
        "s_xor_b32 s57, s3, 2\n"
        "s_mul_i32 s58, s57, 229376\n"
        "buffer_load_dwordx4 v108, s[12:15], s58 offen lds\n"
        "s_waitcnt lgkmcnt(2)\n"
        "v_mfma_f32_16x16x32_bf16 v[16:19], v[208:211], v[88:91], v[16:19]\n"
        "ds_read_b128 v[216:219], v111 offset:0\n"
        "ds_read_b64_tr_b16 v[112:113], v48 offset:4096\n"
        "v_mfma_f32_16x16x32_bf16 v[20:23], v[212:215], v[88:91], v[20:23]\n"
        "ds_read_b128 v[220:223], v111 offset:2048\n"
        "ds_read_b64_tr_b16 v[114:115], v48 offset:4608\n"
        "s_setprio 2\n"
        "v_mfma_f32_16x16x32_bf16 v[24:27], v[64:67], v[88:91], v[24:27]\n"
        "ds_read_b128 v[72:75], v63 offset:0\n"
        "v_mfma_f32_16x16x32_bf16 v[28:31], v[68:71], v[88:91], v[28:31]\n"
        "ds_read_b128 v[76:79], v63 offset:2048\n"
        "s_add_u32 m0, s53, 147456\n"
        "s_nop 0\n"
        "buffer_load_dwordx4 v108, s[16:19], s58 offen lds\n"
        "s_waitcnt lgkmcnt(6)\n"
        "v_mfma_f32_16x16x32_bf16 v[32:35], v[208:211], v[92:95], v[32:35]\n"
        "ds_read_b64_tr_b16 v[80:81], v48 offset:45056\n"
        "v_mfma_f32_16x16x32_bf16 v[36:39], v[212:215], v[92:95], v[36:39]\n"
        "ds_read_b64_tr_b16 v[82:83], v48 offset:45568\n"
        "v_mfma_f32_16x16x32_bf16 v[40:43], v[64:67], v[92:95], v[40:43]\n"
        "v_mfma_f32_16x16x32_bf16 v[44:47], v[68:71], v[92:95], v[44:47]\n"
        "s_add_u32 m0, s53, 73728\n"
        "s_xor_b32 s57, s3, 3\n"
        "s_mul_i32 s58, s57, 229376\n"
        "buffer_load_dwordx4 v108, s[12:15], s58 offen lds\n"
        "s_setprio 1\n"
        "s_waitcnt lgkmcnt(4)\n"
        "v_mfma_f32_16x16x32_bf16 v[0:3], v[112:115], v[216:219], v[0:3]\n"
        "ds_read_b64_tr_b16 v[84:85], v49 offset:4096\n"
        "v_mfma_f32_16x16x32_bf16 v[4:7], v[112:115], v[220:223], v[4:7]\n"
        "ds_read_b64_tr_b16 v[86:87], v49 offset:4608\n"
        "s_waitcnt lgkmcnt(4)\n"
        "v_mfma_f32_16x16x32_bf16 v[8:11], v[112:115], v[72:75], v[8:11]\n"
        "v_mfma_f32_16x16x32_bf16 v[12:15], v[112:115], v[76:79], v[12:15]\n"
        "s_add_u32 m0, s53, 81920\n"
        "s_nop 0\n"
        "buffer_load_dwordx4 v108, s[16:19], s58 offen lds\n"
        "s_setprio 0\n"
        "s_waitcnt lgkmcnt(2)\n"
        "v_mfma_f32_16x16x32_bf16 v[16:19], v[216:219], v[80:83], v[16:19]\n"
        "s_waitcnt vmcnt(8) lgkmcnt(0)\n"
        "s_barrier\n"
        "ds_read_b128 v[240:243], v110 offset:16384\n"
        "ds_read_b64_tr_b16 v[88:89], v48 offset:8192\n"
        "v_mfma_f32_16x16x32_bf16 v[20:23], v[220:223], v[80:83], v[20:23]\n"
        "ds_read_b128 v[244:247], v110 offset:18432\n"
        "ds_read_b64_tr_b16 v[90:91], v48 offset:8704\n"
        "s_setprio 3\n"
        "v_mfma_f32_16x16x32_bf16 v[24:27], v[72:75], v[80:83], v[24:27]\n"
        "ds_read_b128 v[64:67], v62 offset:16384\n"
        "v_mfma_f32_16x16x32_bf16 v[28:31], v[76:79], v[80:83], v[28:31]\n"
        "ds_read_b128 v[68:71], v62 offset:18432\n"
        "s_mov_b32 m0, s53\n"
        "s_xor_b32 s57, s3, 1\n"
        "s_mul_i32 s58, s57, 229376\n"
        "buffer_load_dwordx4 v108, s[12:15], s58 offen lds\n"
        "v_mfma_f32_16x16x32_bf16 v[32:35], v[216:219], v[84:87], v[32:35]\n"
        "ds_read_b128 v[248:251], v111 offset:16384\n"
        "ds_read_b64_tr_b16 v[92:93], v48 offset:12288\n"
        "v_mfma_f32_16x16x32_bf16 v[36:39], v[220:223], v[84:87], v[36:39]\n"
        "ds_read_b128 v[252:255], v111 offset:18432\n"
        "ds_read_b64_tr_b16 v[94:95], v48 offset:12800\n"
        "v_mfma_f32_16x16x32_bf16 v[40:43], v[72:75], v[84:87], v[40:43]\n"
        "ds_read_b128 v[72:75], v63 offset:16384\n"
        "v_mfma_f32_16x16x32_bf16 v[44:47], v[76:79], v[84:87], v[44:47]\n"
        "ds_read_b128 v[76:79], v63 offset:18432\n"
        "s_add_u32 m0, s53, 8192\n"
        "s_nop 0\n"
        "buffer_load_dwordx4 v108, s[16:19], s58 offen lds\n"
        "s_waitcnt lgkmcnt(8)\n"
        "v_mfma_f32_16x16x32_bf16 v[0:3], v[88:91], v[240:243], v[0:3]\n"
        "ds_read_b64_tr_b16 v[112:113], v49 offset:8192\n"
        "v_mfma_f32_16x16x32_bf16 v[4:7], v[88:91], v[244:247], v[4:7]\n"
        "ds_read_b64_tr_b16 v[114:115], v49 offset:8704\n"
        "s_waitcnt lgkmcnt(8)\n"
        "v_mfma_f32_16x16x32_bf16 v[8:11], v[88:91], v[64:67], v[8:11]\n"
        "v_mfma_f32_16x16x32_bf16 v[12:15], v[88:91], v[68:71], v[12:15]\n"
        "s_add_u32 m0, s53, 90112\n"
        "s_xor_b32 s57, s3, 4\n"
        "s_mul_i32 s58, s57, 229376\n"
        "buffer_load_dwordx4 v108, s[12:15], s58 offen lds\n"
        "s_waitcnt lgkmcnt(4)\n"
        "v_mfma_f32_16x16x32_bf16 v[0:3], v[92:95], v[248:251], v[0:3]\n"
        "ds_read_b64_tr_b16 v[80:81], v49 offset:12288\n"
        "v_mfma_f32_16x16x32_bf16 v[4:7], v[92:95], v[252:255], v[4:7]\n"
        "ds_read_b64_tr_b16 v[82:83], v49 offset:12800\n"
        "s_setprio 2\n"
        "s_mov_b32 s40, s65\n"
        "v_lshrrev_b32 v104, 1, v124\n"
        "v_xor_b32 v104, v104, v124\n"
        "v_and_b32 v104, 0x140, v104\n"
        "v_mul_u32_u24 v104, 3, v104\n"
        "s_waitcnt lgkmcnt(4)\n"
        "v_mfma_f32_16x16x32_bf16 v[8:11], v[92:95], v[72:75], v[8:11]\n"
        "v_xor_b32 v105, v104, v124\n"
        "s_add_u32 s42, s63, 0x80000\n"
        "s_add_u32 s43, s51, 0x80000\n"
        "v_mfma_f32_16x16x32_bf16 v[12:15], v[92:95], v[76:79], v[12:15]\n"
        "s_add_u32 m0, s53, 98304\n"
        "s_nop 0\n"
        "buffer_load_dwordx4 v108, s[16:19], s58 offen lds\n"
        "s_waitcnt lgkmcnt(2)\n"
        "v_mfma_f32_16x16x32_bf16 v[16:19], v[240:243], v[112:115], v[16:19]\n"
        "ds_read_b64_tr_b16 v[84:85], v49 offset:16384\n"
        "v_mfma_f32_16x16x32_bf16 v[20:23], v[244:247], v[112:115], v[20:23]\n"
        "ds_read_b64_tr_b16 v[86:87], v49 offset:16896\n"
        "v_mfma_f32_16x16x32_bf16 v[24:27], v[64:67], v[112:115], v[24:27]\n"
        "v_mfma_f32_16x16x32_bf16 v[28:31], v[68:71], v[112:115], v[28:31]\n"
        "s_add_u32 m0, s53, 32768\n"
        "s_xor_b32 s57, s3, 6\n"
        "s_mul_i32 s58, s57, 229376\n"
        "buffer_load_dwordx4 v108, s[12:15], s58 offen lds\n"
        "s_setprio 1\n"
        "s_waitcnt lgkmcnt(2)\n"
        "v_mfma_f32_16x16x32_bf16 v[16:19], v[248:251], v[80:83], v[16:19]\n"
        "ds_read_b64_tr_b16 v[88:89], v49 offset:20480\n"
        "v_mfma_f32_16x16x32_bf16 v[20:23], v[252:255], v[80:83], v[20:23]\n"
        "ds_read_b64_tr_b16 v[90:91], v49 offset:20992\n"
        "v_pk_mul_f32 v[0:1], v[0:1], s[40:41] op_sel_hi:[1,0]\n"
        "v_pk_mul_f32 v[2:3], v[2:3], s[40:41] op_sel_hi:[1,0]\n"
        "v_pk_mul_f32 v[4:5], v[4:5], s[40:41] op_sel_hi:[1,0]\n"
        "v_pk_mul_f32 v[6:7], v[6:7], s[40:41] op_sel_hi:[1,0]\n"
        "v_mfma_f32_16x16x32_bf16 v[24:27], v[72:75], v[80:83], v[24:27]\n"
        "v_cvt_pk_bf16_f32 v0, v0, v1\n"
        "v_cvt_pk_bf16_f32 v1, v2, v3\n"
        "v_cvt_pk_bf16_f32 v2, v4, v5\n"
        "v_cvt_pk_bf16_f32 v3, v6, v7\n"
        "v_mfma_f32_16x16x32_bf16 v[28:31], v[76:79], v[80:83], v[28:31]\n"
        "s_add_u32 m0, s53, 40960\n"
        "s_nop 0\n"
        "buffer_load_dwordx4 v108, s[16:19], s58 offen lds\n"
        "s_setprio 0\n"
        "s_nop 0\n"
        "v_permlane16_swap_b32 v0, v2\n"
        "v_permlane16_swap_b32 v1, v3\n"
        "s_waitcnt lgkmcnt(2)\n"
        "v_mfma_f32_16x16x32_bf16 v[32:35], v[240:243], v[84:87], v[32:35]\n"
        "s_waitcnt vmcnt(4) lgkmcnt(0)\n"
        "s_barrier\n"
        "ds_read_b64_tr_b16 v[92:93], v120 offset:24576\n"
        "buffer_store_dwordx4 v[0:3], v124, s[28:31], s61 offen sc1\n"
        "v_mfma_f32_16x16x32_bf16 v[36:39], v[244:247], v[84:87], v[36:39]\n"
        "ds_read_b64_tr_b16 v[94:95], v120 offset:25088\n"
        "s_setprio 3\n"
        "v_pk_mul_f32 v[8:9], v[8:9], s[40:41] op_sel_hi:[1,0]\n"
        "v_pk_mul_f32 v[10:11], v[10:11], s[40:41] op_sel_hi:[1,0]\n"
        "v_pk_mul_f32 v[12:13], v[12:13], s[40:41] op_sel_hi:[1,0]\n"
        "v_pk_mul_f32 v[14:15], v[14:15], s[40:41] op_sel_hi:[1,0]\n"
        "v_mfma_f32_16x16x32_bf16 v[40:43], v[64:67], v[84:87], v[40:43]\n"
        "ds_read_b64_tr_b16 v[112:113], v121 offset:24576\n"
        "v_cvt_pk_bf16_f32 v4, v8, v9\n"
        "v_cvt_pk_bf16_f32 v5, v10, v11\n"
        "v_cvt_pk_bf16_f32 v6, v12, v13\n"
        "v_cvt_pk_bf16_f32 v7, v14, v15\n"
        "v_mfma_f32_16x16x32_bf16 v[44:47], v[68:71], v[84:87], v[44:47]\n"
        "ds_read_b64_tr_b16 v[114:115], v121 offset:25088\n"
        "s_nop 0\n"
        "v_permlane16_swap_b32 v4, v6\n"
        "v_permlane16_swap_b32 v5, v7\n"
        "v_mfma_f32_16x16x32_bf16 v[32:35], v[248:251], v[88:91], v[32:35]\n"
        "ds_read_b64_tr_b16 v[80:81], v120 offset:32768\n"
        "buffer_store_dwordx4 v[4:7], v124, s[28:31], s62 offen sc1\n"
        "v_mfma_f32_16x16x32_bf16 v[36:39], v[252:255], v[88:91], v[36:39]\n"
        "ds_read_b64_tr_b16 v[82:83], v120 offset:33280\n"
        "v_cvt_pk_bf16_f32 v16, v16, v17\n"
        "v_cvt_pk_bf16_f32 v17, v18, v19\n"
        "v_cvt_pk_bf16_f32 v18, v20, v21\n"
        "v_cvt_pk_bf16_f32 v19, v22, v23\n"
        "v_mfma_f32_16x16x32_bf16 v[40:43], v[72:75], v[88:91], v[40:43]\n"
        "ds_read_b64_tr_b16 v[84:85], v121 offset:32768\n"
        "s_nop 0\n"
        "v_permlane16_swap_b32 v16, v18\n"
        "v_permlane16_swap_b32 v17, v19\n"
        "v_mfma_f32_16x16x32_bf16 v[44:47], v[76:79], v[88:91], v[44:47]\n"
        "ds_read_b64_tr_b16 v[86:87], v121 offset:33280\n"
        "buffer_store_dwordx4 v[16:19], v105, s[28:31], s63 offen sc1\n"
        "s_waitcnt lgkmcnt(4)\n"
        "v_mfma_f32_16x16x32_bf16 v[48:51], v[92:95], v[128:131], 0\n"
        "ds_read_b64_tr_b16 v[88:89], v120 offset:28672\n"
        "v_mfma_f32_16x16x32_bf16 v[52:55], v[92:95], v[132:135], 0\n"
        "ds_read_b64_tr_b16 v[90:91], v120 offset:29184\n"
        "v_cvt_pk_bf16_f32 v24, v24, v25\n"
        "v_cvt_pk_bf16_f32 v25, v26, v27\n"
        "v_cvt_pk_bf16_f32 v26, v28, v29\n"
        "v_cvt_pk_bf16_f32 v27, v30, v31\n"
        "v_mfma_f32_16x16x32_bf16 v[64:67], v[112:115], v[128:131], 0\n"
        "ds_read_b64_tr_b16 v[92:93], v121 offset:28672\n"
        "s_nop 0\n"
        "v_permlane16_swap_b32 v24, v26\n"
        "v_permlane16_swap_b32 v25, v27\n"
        "v_mfma_f32_16x16x32_bf16 v[68:71], v[112:115], v[132:135], 0\n"
        "ds_read_b64_tr_b16 v[94:95], v121 offset:29184\n"
        "buffer_store_dwordx4 v[24:27], v105, s[28:31], s51 offen sc1\n"
        "s_waitcnt lgkmcnt(4)\n"
        "v_mfma_f32_16x16x32_bf16 v[56:59], v[80:83], v[128:131], 0\n"
        "ds_read_b64_tr_b16 v[112:113], v120 offset:36864\n"
        "v_mfma_f32_16x16x32_bf16 v[60:63], v[80:83], v[132:135], 0\n"
        "ds_read_b64_tr_b16 v[114:115], v120 offset:37376\n"
        "v_cvt_pk_bf16_f32 v32, v32, v33\n"
        "v_cvt_pk_bf16_f32 v33, v34, v35\n"
        "v_cvt_pk_bf16_f32 v34, v36, v37\n"
        "v_cvt_pk_bf16_f32 v35, v38, v39\n"
        "v_mfma_f32_16x16x32_bf16 v[72:75], v[84:87], v[128:131], 0\n"
        "ds_read_b64_tr_b16 v[80:81], v121 offset:36864\n"
        "s_nop 0\n"
        "v_permlane16_swap_b32 v32, v34\n"
        "v_permlane16_swap_b32 v33, v35\n"
        "v_mfma_f32_16x16x32_bf16 v[76:79], v[84:87], v[132:135], 0\n"
        "ds_read_b64_tr_b16 v[82:83], v121 offset:37376\n"
        "buffer_store_dwordx4 v[32:35], v105, s[28:31], s42 offen sc1\n"
        "s_waitcnt lgkmcnt(4)\n"
        "v_mfma_f32_16x16x32_bf16 v[48:51], v[88:91], v[136:139], v[48:51]\n"
        "ds_read_b64_tr_b16 v[84:85], v120 offset:40960\n"
        "v_mfma_f32_16x16x32_bf16 v[52:55], v[88:91], v[140:143], v[52:55]\n"
        "ds_read_b64_tr_b16 v[86:87], v120 offset:41472\n"
        "v_cvt_pk_bf16_f32 v40, v40, v41\n"
        "v_cvt_pk_bf16_f32 v41, v42, v43\n"
        "v_cvt_pk_bf16_f32 v42, v44, v45\n"
        "v_cvt_pk_bf16_f32 v43, v46, v47\n"
        "v_mfma_f32_16x16x32_bf16 v[64:67], v[92:95], v[136:139], v[64:67]\n"
        "ds_read_b64_tr_b16 v[88:89], v121 offset:40960\n"
        "s_nop 0\n"
        "v_permlane16_swap_b32 v40, v42\n"
        "v_permlane16_swap_b32 v41, v43\n"
        "v_mfma_f32_16x16x32_bf16 v[68:71], v[92:95], v[140:143], v[68:71]\n"
        "ds_read_b64_tr_b16 v[90:91], v121 offset:41472\n"
        "buffer_store_dwordx4 v[40:43], v105, s[28:31], s43 offen sc1\n"
        "s_waitcnt lgkmcnt(4)\n"
        "v_mfma_f32_16x16x32_bf16 v[56:59], v[112:115], v[136:139], v[56:59]\n"
        "ds_read_b64_tr_b16 v[92:93], v120 offset:49152\n"
        "v_mfma_f32_16x16x32_bf16 v[60:63], v[112:115], v[140:143], v[60:63]\n"
        "ds_read_b64_tr_b16 v[94:95], v120 offset:49664\n"
        "v_mfma_f32_16x16x32_bf16 v[72:75], v[80:83], v[136:139], v[72:75]\n"
        "ds_read_b64_tr_b16 v[112:113], v121 offset:49152\n"
        "v_mfma_f32_16x16x32_bf16 v[76:79], v[80:83], v[140:143], v[76:79]\n"
        "ds_read_b64_tr_b16 v[114:115], v121 offset:49664\n"
        "s_waitcnt lgkmcnt(4)\n"
        "v_mfma_f32_16x16x32_bf16 v[48:51], v[84:87], v[144:147], v[48:51]\n"
        "ds_read_b64_tr_b16 v[80:81], v120 offset:45056\n"
        "v_mfma_f32_16x16x32_bf16 v[52:55], v[84:87], v[148:151], v[52:55]\n"
        "ds_read_b64_tr_b16 v[82:83], v120 offset:45568\n"
        "s_setprio 2\n"
        "v_mfma_f32_16x16x32_bf16 v[64:67], v[88:91], v[144:147], v[64:67]\n"
        "ds_read_b64_tr_b16 v[84:85], v121 offset:45056\n"
        "v_mfma_f32_16x16x32_bf16 v[68:71], v[88:91], v[148:151], v[68:71]\n"
        "ds_read_b64_tr_b16 v[86:87], v121 offset:45568\n"
        "s_waitcnt lgkmcnt(4)\n"
        "v_mfma_f32_16x16x32_bf16 v[56:59], v[92:95], v[144:147], v[56:59]\n"
        "ds_read_b64_tr_b16 v[88:89], v120 offset:53248\n"
        "v_mfma_f32_16x16x32_bf16 v[60:63], v[92:95], v[148:151], v[60:63]\n"
        "ds_read_b64_tr_b16 v[90:91], v120 offset:53760\n"
        "v_mfma_f32_16x16x32_bf16 v[72:75], v[112:115], v[144:147], v[72:75]\n"
        "ds_read_b64_tr_b16 v[92:93], v121 offset:53248\n"
        "v_mfma_f32_16x16x32_bf16 v[76:79], v[112:115], v[148:151], v[76:79]\n"
        "ds_read_b64_tr_b16 v[94:95], v121 offset:53760\n"
        "s_waitcnt lgkmcnt(4)\n"
        "v_mfma_f32_16x16x32_bf16 v[48:51], v[80:83], v[152:155], v[48:51]\n"
        "ds_read_b64_tr_b16 v[112:113], v116 offset:0\n"
        "v_mfma_f32_16x16x32_bf16 v[52:55], v[80:83], v[156:159], v[52:55]\n"
        "ds_read_b64_tr_b16 v[114:115], v116 offset:512\n"
        "v_mfma_f32_16x16x32_bf16 v[64:67], v[84:87], v[152:155], v[64:67]\n"
        "ds_read_b64_tr_b16 v[80:81], v117 offset:0\n"
        "v_mfma_f32_16x16x32_bf16 v[68:71], v[84:87], v[156:159], v[68:71]\n"
        "ds_read_b64_tr_b16 v[82:83], v117 offset:512\n"
        "s_waitcnt lgkmcnt(4)\n"
        "v_mfma_f32_16x16x32_bf16 v[56:59], v[88:91], v[152:155], v[56:59]\n"
        "ds_read_b64_tr_b16 v[84:85], v116 offset:8192\n"
        "v_mfma_f32_16x16x32_bf16 v[60:63], v[88:91], v[156:159], v[60:63]\n"
        "ds_read_b64_tr_b16 v[86:87], v116 offset:8704\n"
        "s_setprio 1\n"
        "v_mfma_f32_16x16x32_bf16 v[72:75], v[92:95], v[152:155], v[72:75]\n"
        "ds_read_b64_tr_b16 v[88:89], v117 offset:8192\n"
        "v_mfma_f32_16x16x32_bf16 v[76:79], v[92:95], v[156:159], v[76:79]\n"
        "ds_read_b64_tr_b16 v[90:91], v117 offset:8704\n"
        "s_waitcnt lgkmcnt(4)\n"
        "v_mfma_f32_16x16x32_bf16 v[48:51], v[112:115], v[160:163], v[48:51]\n"
        "ds_read_b64_tr_b16 v[92:93], v116 offset:4096\n"
        "v_mfma_f32_16x16x32_bf16 v[52:55], v[112:115], v[164:167], v[52:55]\n"
        "ds_read_b64_tr_b16 v[94:95], v116 offset:4608\n"
        "v_mfma_f32_16x16x32_bf16 v[64:67], v[80:83], v[160:163], v[64:67]\n"
        "ds_read_b64_tr_b16 v[112:113], v117 offset:4096\n"
        "v_mfma_f32_16x16x32_bf16 v[68:71], v[80:83], v[164:167], v[68:71]\n"
        "ds_read_b64_tr_b16 v[114:115], v117 offset:4608\n"
        "s_waitcnt lgkmcnt(4)\n"
        "v_mfma_f32_16x16x32_bf16 v[56:59], v[84:87], v[160:163], v[56:59]\n"
        "ds_read_b64_tr_b16 v[80:81], v116 offset:12288\n"
        "v_mfma_f32_16x16x32_bf16 v[60:63], v[84:87], v[164:167], v[60:63]\n"
        "ds_read_b64_tr_b16 v[82:83], v116 offset:12800\n"
        "v_mfma_f32_16x16x32_bf16 v[72:75], v[88:91], v[160:163], v[72:75]\n"
        "ds_read_b64_tr_b16 v[84:85], v117 offset:12288\n"
        "v_mfma_f32_16x16x32_bf16 v[76:79], v[88:91], v[164:167], v[76:79]\n"
        "ds_read_b64_tr_b16 v[86:87], v117 offset:12800\n"
        "s_setprio 0\n"
        "s_waitcnt lgkmcnt(4)\n"
        "v_mfma_f32_16x16x32_bf16 v[48:51], v[92:95], v[168:171], v[48:51]\n"
        "s_waitcnt vmcnt(0) lgkmcnt(0)\n"
        "s_barrier\n"
        "s_cmp_lg_u32 s52, 0\n"
        "s_cbranch_scc1 LPB%=\n"
        "s_mov_b64 exec, 1\n"
        "v_mov_b32 v106, s48\n"
        "v_mov_b32 v107, s49\n"
        "buffer_store_dwordx2 v[106:107], off, s[28:31], s66 sc1\n"
        "s_mov_b64 exec, -1\n"
        "LPB%=:\n"
        "ds_read_b64_tr_b16 v[88:89], v118 offset:40960\n"
        "v_mfma_f32_16x16x32_bf16 v[52:55], v[92:95], v[172:175], v[52:55]\n"
        "ds_read_b64_tr_b16 v[90:91], v118 offset:41472\n"
        "s_add_u32 m0, s53, 106496\n"
        "s_xor_b32 s57, s3, 5\n"
        "s_mul_i32 s58, s57, 229376\n"
        "buffer_load_dwordx4 v108, s[12:15], s58 offen lds\n"
        "s_setprio 3\n"
        "v_mfma_f32_16x16x32_bf16 v[64:67], v[112:115], v[168:171], v[64:67]\n"
        "ds_read_b64_tr_b16 v[92:93], v119 offset:40960\n"
        "v_mfma_f32_16x16x32_bf16 v[68:71], v[112:115], v[172:175], v[68:71]\n"
        "ds_read_b64_tr_b16 v[94:95], v119 offset:41472\n"
        "s_add_u32 m0, s53, 114688\n"
        "s_nop 0\n"
        "buffer_load_dwordx4 v108, s[16:19], s58 offen lds\n"
        "v_mfma_f32_16x16x32_bf16 v[56:59], v[80:83], v[168:171], v[56:59]\n"
        "ds_read_b64_tr_b16 v[112:113], v120 offset:0\n"
        "v_mfma_f32_16x16x32_bf16 v[60:63], v[80:83], v[172:175], v[60:63]\n"
        "ds_read_b64_tr_b16 v[114:115], v120 offset:512\n"
        "s_add_u32 m0, s53, 49152\n"
        "s_xor_b32 s57, s3, 7\n"
        "s_mul_i32 s58, s57, 229376\n"
        "buffer_load_dwordx4 v108, s[12:15], s58 offen lds\n"
        "buffer_load_dwordx4 v[160:163], v124, s[4:7], s71 offen\n"
        "v_mfma_f32_16x16x32_bf16 v[72:75], v[84:87], v[168:171], v[72:75]\n"
        "ds_read_b64_tr_b16 v[80:81], v121 offset:0\n"
        "v_mfma_f32_16x16x32_bf16 v[76:79], v[84:87], v[172:175], v[76:79]\n"
        "ds_read_b64_tr_b16 v[82:83], v121 offset:512\n"
        "s_add_u32 m0, s53, 57344\n"
        "s_nop 0\n"
        "buffer_load_dwordx4 v108, s[16:19], s58 offen lds\n"
        "buffer_load_dwordx4 v[164:167], v124, s[36:39], 0 offen\n"
        "s_waitcnt lgkmcnt(4)\n"
        "v_mfma_f32_16x16x32_bf16 v[48:51], v[88:91], v[176:179], v[48:51]\n"
        "ds_read_b64_tr_b16 v[84:85], v118 offset:45056\n"
        "v_mfma_f32_16x16x32_bf16 v[52:55], v[88:91], v[180:183], v[52:55]\n"
        "ds_read_b64_tr_b16 v[86:87], v118 offset:45568\n"
        "v_mov_b64 v[168:169], 0\n"
        "v_mov_b64 v[170:171], 0\n"
        "s_nop 1\n"
        "v_mfma_f32_32x32x16_bf16 v[0:15], v[168:171], v[168:171], 0\n"
        "v_mfma_f32_16x16x32_bf16 v[64:67], v[92:95], v[176:179], v[64:67]\n"
        "ds_read_b64_tr_b16 v[88:89], v119 offset:45056\n"
        "v_mfma_f32_32x32x16_bf16 v[16:31], v[168:171], v[168:171], 0\n"
        "v_mfma_f32_16x16x32_bf16 v[68:71], v[92:95], v[180:183], v[68:71]\n"
        "ds_read_b64_tr_b16 v[90:91], v119 offset:45568\n"
        "s_waitcnt lgkmcnt(4)\n"
        "v_mfma_f32_16x16x32_bf16 v[56:59], v[112:115], v[176:179], v[56:59]\n"
        "ds_read_b64_tr_b16 v[92:93], v120 offset:4096\n"
        "v_mfma_f32_16x16x32_bf16 v[60:63], v[112:115], v[180:183], v[60:63]\n"
        "ds_read_b64_tr_b16 v[94:95], v120 offset:4608\n"
        "v_mfma_f32_16x16x32_bf16 v[72:75], v[80:83], v[176:179], v[72:75]\n"
        "ds_read_b64_tr_b16 v[112:113], v121 offset:4096\n"
        "v_mfma_f32_16x16x32_bf16 v[76:79], v[80:83], v[180:183], v[76:79]\n"
        "ds_read_b64_tr_b16 v[114:115], v121 offset:4608\n"
        "s_waitcnt lgkmcnt(4)\n"
        "v_mfma_f32_16x16x32_bf16 v[48:51], v[84:87], v[184:187], v[48:51]\n"
        "ds_read_b64_tr_b16 v[80:81], v118 offset:24576\n"
        "v_mfma_f32_16x16x32_bf16 v[52:55], v[84:87], v[188:191], v[52:55]\n"
        "ds_read_b64_tr_b16 v[82:83], v118 offset:25088\n"
        "v_mfma_f32_16x16x32_bf16 v[64:67], v[88:91], v[184:187], v[64:67]\n"
        "ds_read_b64_tr_b16 v[84:85], v119 offset:24576\n"
        "v_mfma_f32_16x16x32_bf16 v[68:71], v[88:91], v[188:191], v[68:71]\n"
        "ds_read_b64_tr_b16 v[86:87], v119 offset:25088\n"
        "s_waitcnt lgkmcnt(4)\n"
        "v_mfma_f32_16x16x32_bf16 v[56:59], v[92:95], v[184:187], v[56:59]\n"
        "ds_read_b64_tr_b16 v[88:89], v118 offset:32768\n"
        "v_mfma_f32_16x16x32_bf16 v[60:63], v[92:95], v[188:191], v[60:63]\n"
        "ds_read_b64_tr_b16 v[90:91], v118 offset:33280\n"
        "s_setprio 2\n"
        "v_mfma_f32_16x16x32_bf16 v[72:75], v[112:115], v[184:187], v[72:75]\n"
        "ds_read_b64_tr_b16 v[92:93], v119 offset:32768\n"
        "v_mfma_f32_16x16x32_bf16 v[76:79], v[112:115], v[188:191], v[76:79]\n"
        "ds_read_b64_tr_b16 v[94:95], v119 offset:33280\n"
        "s_waitcnt lgkmcnt(4)\n"
        "v_mfma_f32_16x16x32_bf16 v[48:51], v[80:83], v[192:195], v[48:51]\n"
        "ds_read_b64_tr_b16 v[112:113], v118 offset:28672\n"
        "v_mfma_f32_16x16x32_bf16 v[52:55], v[80:83], v[196:199], v[52:55]\n"
        "ds_read_b64_tr_b16 v[114:115], v118 offset:29184\n"
        "v_mfma_f32_16x16x32_bf16 v[64:67], v[84:87], v[192:195], v[64:67]\n"
        "ds_read_b64_tr_b16 v[80:81], v119 offset:28672\n"
        "v_mfma_f32_16x16x32_bf16 v[68:71], v[84:87], v[196:199], v[68:71]\n"
        "ds_read_b64_tr_b16 v[82:83], v119 offset:29184\n"
        "s_waitcnt lgkmcnt(4)\n"
        "v_mfma_f32_16x16x32_bf16 v[56:59], v[88:91], v[192:195], v[56:59]\n"
        "ds_read_b64_tr_b16 v[84:85], v118 offset:36864\n"
        "v_mfma_f32_16x16x32_bf16 v[60:63], v[88:91], v[196:199], v[60:63]\n"
        "ds_read_b64_tr_b16 v[86:87], v118 offset:37376\n"
        "v_mfma_f32_16x16x32_bf16 v[72:75], v[92:95], v[192:195], v[72:75]\n"
        "ds_read_b64_tr_b16 v[88:89], v119 offset:36864\n"
        "v_mfma_f32_16x16x32_bf16 v[76:79], v[92:95], v[196:199], v[76:79]\n"
        "ds_read_b64_tr_b16 v[90:91], v119 offset:37376\n"
        "s_setprio 1\n"
        "s_waitcnt lgkmcnt(4)\n"
        "v_mfma_f32_16x16x32_bf16 v[48:51], v[112:115], v[200:203], v[48:51]\n"
        "ds_read_b64_tr_b16 v[92:93], v116 offset:32768\n"
        "v_mfma_f32_16x16x32_bf16 v[52:55], v[112:115], v[204:207], v[52:55]\n"
        "ds_read_b64_tr_b16 v[94:95], v116 offset:33280\n"
        "v_mfma_f32_16x16x32_bf16 v[64:67], v[80:83], v[200:203], v[64:67]\n"
        "ds_read_b64_tr_b16 v[112:113], v116 offset:40960\n"
        "v_mfma_f32_16x16x32_bf16 v[68:71], v[80:83], v[204:207], v[68:71]\n"
        "ds_read_b64_tr_b16 v[114:115], v116 offset:41472\n"
        "s_waitcnt lgkmcnt(4)\n"
        "v_mfma_f32_16x16x32_bf16 v[56:59], v[84:87], v[200:203], v[56:59]\n"
        "ds_read_b64_tr_b16 v[80:81], v116 offset:36864\n"
        "v_mfma_f32_16x16x32_bf16 v[60:63], v[84:87], v[204:207], v[60:63]\n"
        "ds_read_b64_tr_b16 v[82:83], v116 offset:37376\n"
        "v_mfma_f32_16x16x32_bf16 v[72:75], v[88:91], v[200:203], v[72:75]\n"
        "ds_read_b64_tr_b16 v[84:85], v116 offset:45056\n"
        "v_mfma_f32_16x16x32_bf16 v[76:79], v[88:91], v[204:207], v[76:79]\n"
        "ds_read_b64_tr_b16 v[86:87], v116 offset:45568\n"
        "s_setprio 0\n"
        "s_waitcnt lgkmcnt(4)\n"
        "v_mfma_f32_16x16x32_bf16 v[48:51], v[92:95], v[208:211], v[48:51]\n"
        "s_waitcnt vmcnt(1) lgkmcnt(0)\n"
        "s_barrier\n"
        "ds_read_b64_tr_b16 v[88:89], v120 offset:8192\n"
        "v_mfma_f32_16x16x32_bf16 v[52:55], v[92:95], v[212:215], v[52:55]\n"
        "ds_read_b64_tr_b16 v[90:91], v120 offset:8704\n"
        "s_setprio 3\n"
        "v_mfma_f32_16x16x32_bf16 v[56:59], v[112:115], v[208:211], v[56:59]\n"
        "ds_read_b64_tr_b16 v[92:93], v120 offset:16384\n"
        "v_mfma_f32_16x16x32_bf16 v[60:63], v[112:115], v[212:215], v[60:63]\n"
        "ds_read_b64_tr_b16 v[94:95], v120 offset:16896\n"
        "v_mfma_f32_16x16x32_bf16 v[48:51], v[80:83], v[216:219], v[48:51]\n"
        "ds_read_b64_tr_b16 v[112:113], v120 offset:12288\n"
        "v_mfma_f32_16x16x32_bf16 v[52:55], v[80:83], v[220:223], v[52:55]\n"
        "ds_read_b64_tr_b16 v[114:115], v120 offset:12800\n"
        "s_waitcnt vmcnt(0)\n"
        "v_mov_b32 v109, 0\n"
        "v_dot2c_f32_bf16 v109, v160, v164\n"
        "v_dot2c_f32_bf16 v109, v161, v165\n"
        "v_mfma_f32_16x16x32_bf16 v[56:59], v[84:87], v[216:219], v[56:59]\n"
        "ds_read_b64_tr_b16 v[80:81], v120 offset:20480\n"
        "v_dot2c_f32_bf16 v109, v162, v166\n"
        "v_dot2c_f32_bf16 v109, v163, v167\n"
        "s_nop 2\n"
        "v_add_f32_dpp v109, v109, v109 row_shr:8 row_mask:0xf bank_mask:0xf bound_ctrl:1\n"
        "s_nop 1\n"
        "v_mfma_f32_16x16x32_bf16 v[60:63], v[84:87], v[220:223], v[60:63]\n"
        "ds_read_b64_tr_b16 v[82:83], v120 offset:20992\n"
        "v_add_f32_dpp v109, v109, v109 row_shr:4 row_mask:0xf bank_mask:0xf bound_ctrl:1\n"
        "s_nop 1\n"
        "v_add_f32_dpp v109, v109, v109 row_shr:2 row_mask:0xf bank_mask:0xf bound_ctrl:1\n"
        "s_nop 1\n"
        "v_add_f32_dpp v109, v109, v109 row_shr:1 row_mask:0xf bank_mask:0xf bound_ctrl:1\n"
        "s_waitcnt lgkmcnt(4)\n"
        "v_mfma_f32_16x16x32_bf16 v[48:51], v[88:91], v[224:227], v[48:51]\n"
        "ds_read_b64_tr_b16 v[84:85], v118 offset:0\n"
        "s_nop 1\n"
        "v_add_f32_dpp v109, v109, v109 row_bcast:15 row_mask:0xa bank_mask:0xf\n"
        "s_nop 1\n"
        "v_add_f32_dpp v109, v109, v109 row_bcast:31 row_mask:0xc bank_mask:0xf\n"
        "v_mul_f32 v109, 0xbfb8aa3b, v109\n"
        "v_mfma_f32_16x16x32_bf16 v[52:55], v[88:91], v[228:231], v[52:55]\n"
        "ds_read_b64_tr_b16 v[86:87], v118 offset:512\n"
        "v_exp_f32 v109, v109\n"
        "s_nop 0\n"
        "v_add_f32 v109, 1.0, v109\n"
        "v_rcp_f32 v160, v109\n"
        "v_mfma_f32_16x16x32_bf16 v[56:59], v[92:95], v[224:227], v[56:59]\n"
        "ds_read_b64_tr_b16 v[88:89], v118 offset:8192\n"
        "v_mfma_f32_16x16x32_bf16 v[60:63], v[92:95], v[228:231], v[60:63]\n"
        "ds_read_b64_tr_b16 v[90:91], v118 offset:8704\n"
        "s_waitcnt lgkmcnt(4)\n"
        "v_mfma_f32_16x16x32_bf16 v[48:51], v[112:115], v[232:235], v[48:51]\n"
        "ds_read_b64_tr_b16 v[92:93], v118 offset:4096\n"
        "v_mfma_f32_16x16x32_bf16 v[52:55], v[112:115], v[236:239], v[52:55]\n"
        "ds_read_b64_tr_b16 v[94:95], v118 offset:4608\n"
        "buffer_load_dwordx2 v[106:107], v123, s[28:31], 0 offen sc1\n"
        "v_mfma_f32_16x16x32_bf16 v[56:59], v[80:83], v[232:235], v[56:59]\n"
        "ds_read_b64_tr_b16 v[112:113], v118 offset:12288\n"
        "v_mfma_f32_16x16x32_bf16 v[60:63], v[80:83], v[236:239], v[60:63]\n"
        "ds_read_b64_tr_b16 v[114:115], v118 offset:12800\n"
        "s_waitcnt lgkmcnt(4)\n"
        "v_mfma_f32_16x16x32_bf16 v[48:51], v[84:87], v[240:243], v[48:51]\n"
        "ds_read_b64_tr_b16 v[80:81], v117 offset:32768\n"
        "v_mfma_f32_16x16x32_bf16 v[52:55], v[84:87], v[244:247], v[52:55]\n"
        "ds_read_b64_tr_b16 v[82:83], v117 offset:33280\n"
        "v_mfma_f32_16x16x32_bf16 v[56:59], v[88:91], v[240:243], v[56:59]\n"
        "ds_read_b64_tr_b16 v[84:85], v117 offset:40960\n"
        "v_mfma_f32_16x16x32_bf16 v[60:63], v[88:91], v[244:247], v[60:63]\n"
        "ds_read_b64_tr_b16 v[86:87], v117 offset:41472\n"
        "s_waitcnt lgkmcnt(4)\n"
        "v_mfma_f32_16x16x32_bf16 v[48:51], v[92:95], v[248:251], v[48:51]\n"
        "ds_read_b64_tr_b16 v[88:89], v117 offset:36864\n"
        "v_mfma_f32_16x16x32_bf16 v[52:55], v[92:95], v[252:255], v[52:55]\n"
        "ds_read_b64_tr_b16 v[90:91], v117 offset:37376\n"
        "v_mfma_f32_16x16x32_bf16 v[56:59], v[112:115], v[248:251], v[56:59]\n"
        "ds_read_b64_tr_b16 v[92:93], v117 offset:45056\n"
        "v_mfma_f32_16x16x32_bf16 v[60:63], v[112:115], v[252:255], v[60:63]\n"
        "ds_read_b64_tr_b16 v[94:95], v117 offset:45568\n"
        "s_waitcnt lgkmcnt(4)\n"
        "v_mfma_f32_16x16x32_bf16 v[64:67], v[80:83], v[208:211], v[64:67]\n"
        "ds_read_b64_tr_b16 v[112:113], v121 offset:8192\n"
        "v_mfma_f32_16x16x32_bf16 v[68:71], v[80:83], v[212:215], v[68:71]\n"
        "ds_read_b64_tr_b16 v[114:115], v121 offset:8704\n"
        "s_waitcnt vmcnt(0)\n"
        "v_cmp_ne_u64_e64 vcc, v[106:107], s[48:49]\n"
        "s_mov_b32 s77, 0x100000\n"
        "s_cmp_eq_u64 vcc, 0\n"
        "s_cbranch_scc1 LCBQ%=\n"
        "LCBP%=:\n"
        "s_sleep 1\n"
        "buffer_load_dwordx2 v[106:107], v123, s[28:31], 0 offen sc1\n"
        "s_sub_u32 s77, s77, 1\n"
        "s_cmp_eq_u32 s77, 0\n"
        "s_waitcnt vmcnt(0)\n"
        "s_cbranch_scc1 LCBQ%=\n"
        "v_cmp_ne_u64_e64 vcc, v[106:107], s[48:49]\n"
        "s_nop 0\n"
        "s_cmp_eq_u64 vcc, 0\n"
        "s_cbranch_scc0 LCBP%=\n"
        "LCBQ%=:\n"
        "buffer_load_dwordx4 v[128:131], v124, s[28:31], s69 offen sc1\n"
        "s_setprio 2\n"
        "v_mfma_f32_16x16x32_bf16 v[72:75], v[84:87], v[208:211], v[72:75]\n"
        "ds_read_b64_tr_b16 v[80:81], v121 offset:16384\n"
        "v_mfma_f32_16x16x32_bf16 v[76:79], v[84:87], v[212:215], v[76:79]\n"
        "ds_read_b64_tr_b16 v[82:83], v121 offset:16896\n"
        "buffer_load_dwordx4 v[132:135], v124, s[28:31], s70 offen sc1\n"
        "s_waitcnt lgkmcnt(4)\n"
        "v_mfma_f32_16x16x32_bf16 v[64:67], v[88:91], v[216:219], v[64:67]\n"
        "ds_read_b64_tr_b16 v[84:85], v121 offset:12288\n"
        "v_mfma_f32_16x16x32_bf16 v[68:71], v[88:91], v[220:223], v[68:71]\n"
        "ds_read_b64_tr_b16 v[86:87], v121 offset:12800\n"
        "s_mov_b32 m0, s54\n"
        "s_xor_b32 s57, s94, 0\n"
        "buffer_load_dwordx4 v125, s[28:31], s57 offen sc1 lds\n"
        "v_mul_f32 v172, v48, v48\n"
        "v_mul_f32 v173, v49, v49\n"
        "v_mul_f32 v174, v50, v50\n"
        "v_mul_f32 v175, v51, v51\n"
        "v_mfma_f32_16x16x32_bf16 v[72:75], v[92:95], v[216:219], v[72:75]\n"
        "ds_read_b64_tr_b16 v[88:89], v121 offset:20480\n"
        "v_fma_f32 v172, v172, s50, v122\n"
        "v_fma_f32 v173, v173, s50, v122\n"
        "v_fma_f32 v174, v174, s50, v122\n"
        "v_fma_f32 v175, v175, s50, v122\n"
        "v_mfma_f32_16x16x32_bf16 v[76:79], v[92:95], v[220:223], v[76:79]\n"
        "ds_read_b64_tr_b16 v[90:91], v121 offset:20992\n"
        "s_mov_b32 m0, s54\n"
        "s_xor_b32 s57, s94, 0\n"
        "buffer_load_dwordx4 v125, s[28:31], s57 offen offset:1024 sc1 lds\n"
        "v_mul_f32 v172, v172, v48\n"
        "v_mul_f32 v173, v173, v49\n"
        "v_mul_f32 v174, v174, v50\n"
        "v_mul_f32 v175, v175, v51\n"
        "s_waitcnt lgkmcnt(4)\n"
        "v_mfma_f32_16x16x32_bf16 v[64:67], v[112:115], v[224:227], v[64:67]\n"
        "ds_read_b64_tr_b16 v[92:93], v119 offset:0\n"
        "v_exp_f32 v172, v172\n"
        "v_exp_f32 v173, v173\n"
        "v_mfma_f32_16x16x32_bf16 v[68:71], v[112:115], v[228:231], v[68:71]\n"
        "ds_read_b64_tr_b16 v[94:95], v119 offset:512\n"
        "s_add_u32 m0, s54, 16384\n"
        "s_xor_b32 s57, s94, 16384\n"
        "buffer_load_dwordx4 v125, s[28:31], s57 offen sc1 lds\n"
        "v_exp_f32 v174, v174\n"
        "v_exp_f32 v175, v175\n"
        "v_mfma_f32_16x16x32_bf16 v[72:75], v[80:83], v[224:227], v[72:75]\n"
        "ds_read_b64_tr_b16 v[112:113], v119 offset:8192\n"
        "v_mul_f32 v56, v48, v56\n"
        "v_mul_f32 v57, v49, v57\n"
        "v_mul_f32 v58, v50, v58\n"
        "v_mul_f32 v59, v51, v59\n"
        "v_mfma_f32_16x16x32_bf16 v[76:79], v[80:83], v[228:231], v[76:79]\n"
        "ds_read_b64_tr_b16 v[114:115], v119 offset:8704\n"
        "s_add_u32 m0, s54, 16384\n"
        "s_xor_b32 s57, s94, 16384\n"
        "buffer_load_dwordx4 v125, s[28:31], s57 offen offset:1024 sc1 lds\n"
        "v_add_f32 v172, 1.0, v172\n"
        "v_add_f32 v173, 1.0, v173\n"
        "v_add_f32 v174, 1.0, v174\n"
        "v_add_f32 v175, 1.0, v175\n"
        "s_waitcnt lgkmcnt(4)\n"
        "v_mfma_f32_16x16x32_bf16 v[64:67], v[84:87], v[232:235], v[64:67]\n"
        "ds_read_b64_tr_b16 v[80:81], v119 offset:4096\n"
        "v_rcp_f32 v172, v172\n"
        "v_rcp_f32 v173, v173\n"
        "v_mfma_f32_16x16x32_bf16 v[68:71], v[84:87], v[236:239], v[68:71]\n"
        "ds_read_b64_tr_b16 v[82:83], v119 offset:4608\n"
        "s_setprio 1\n"
        "v_rcp_f32 v174, v174\n"
        "v_rcp_f32 v175, v175\n"
        "v_mfma_f32_16x16x32_bf16 v[72:75], v[88:91], v[232:235], v[72:75]\n"
        "ds_read_b64_tr_b16 v[84:85], v119 offset:12288\n"
        "v_mul_f32 v56, v56, v172\n"
        "v_mul_f32 v57, v57, v173\n"
        "v_mul_f32 v58, v58, v174\n"
        "v_mul_f32 v59, v59, v175\n"
        "v_mfma_f32_16x16x32_bf16 v[76:79], v[88:91], v[236:239], v[76:79]\n"
        "ds_read_b64_tr_b16 v[86:87], v119 offset:12800\n"
        "v_cvt_pk_bf16_f32 v152, v56, v57\n"
        "v_cvt_pk_bf16_f32 v153, v58, v59\n"
        "v_mul_f32 v172, v52, v52\n"
        "v_mul_f32 v173, v53, v53\n"
        "s_waitcnt lgkmcnt(4)\n"
        "v_mfma_f32_16x16x32_bf16 v[64:67], v[92:95], v[240:243], v[64:67]\n"
        "v_mul_f32 v174, v54, v54\n"
        "v_mul_f32 v175, v55, v55\n"
        "v_fma_f32 v172, v172, s50, v122\n"
        "v_fma_f32 v173, v173, s50, v122\n"
        "v_mfma_f32_16x16x32_bf16 v[68:71], v[92:95], v[244:247], v[68:71]\n"
        "v_fma_f32 v174, v174, s50, v122\n"
        "v_fma_f32 v175, v175, s50, v122\n"
        "v_mul_f32 v172, v172, v52\n"
        "v_mul_f32 v173, v173, v53\n"
        "v_mfma_f32_16x16x32_bf16 v[72:75], v[112:115], v[240:243], v[72:75]\n"
        "v_mul_f32 v174, v174, v54\n"
        "v_mul_f32 v175, v175, v55\n"
        "v_exp_f32 v172, v172\n"
        "v_mfma_f32_16x16x32_bf16 v[76:79], v[112:115], v[244:247], v[76:79]\n"
        "v_exp_f32 v173, v173\n"
        "v_exp_f32 v174, v174\n"
        "s_waitcnt lgkmcnt(0)\n"
        "v_mfma_f32_16x16x32_bf16 v[64:67], v[80:83], v[248:251], v[64:67]\n"
        "v_exp_f32 v175, v175\n"
        "v_mul_f32 v60, v52, v60\n"
        "v_mul_f32 v61, v53, v61\n"
        "v_mfma_f32_16x16x32_bf16 v[68:71], v[80:83], v[252:255], v[68:71]\n"
        "s_setprio 0\n"
        "v_mul_f32 v62, v54, v62\n"
        "v_mul_f32 v63, v55, v63\n"
        "v_add_f32 v172, 1.0, v172\n"
        "v_add_f32 v173, 1.0, v173\n"
        "v_mfma_f32_16x16x32_bf16 v[72:75], v[84:87], v[248:251], v[72:75]\n"
        "v_add_f32 v174, 1.0, v174\n"
        "v_add_f32 v175, 1.0, v175\n"
        "v_rcp_f32 v172, v172\n"
        "v_mfma_f32_16x16x32_bf16 v[76:79], v[84:87], v[252:255], v[76:79]\n"
        "v_rcp_f32 v173, v173\n"
        "v_rcp_f32 v174, v174\n"
        "v_rcp_f32 v175, v175\n"
        "v_mul_f32 v60, v60, v172\n"
        "v_mul_f32 v61, v61, v173\n"
        "v_mul_f32 v62, v62, v174\n"
        "v_mul_f32 v63, v63, v175\n"
        "v_cvt_pk_bf16_f32 v154, v60, v61\n"
        "v_cvt_pk_bf16_f32 v155, v62, v63\n"
        "s_nop 0\n"
        "v_permlane16_swap_b32 v152, v154\n"
        "v_permlane16_swap_b32 v153, v155\n"
        "v_pk_mul_f32 v[172:173], v[64:65], v[64:65]\n"
        "v_pk_mul_f32 v[174:175], v[66:67], v[66:67]\n"
        "v_pk_fma_f32 v[172:173], v[172:173], s[50:51], v[122:123] op_sel_hi:[1,0,0]\n"
        "v_pk_fma_f32 v[174:175], v[174:175], s[50:51], v[122:123] op_sel_hi:[1,0,0]\n"
        "v_pk_mul_f32 v[172:173], v[172:173], v[64:65]\n"
        "v_pk_mul_f32 v[174:175], v[174:175], v[66:67]\n"
        "v_exp_f32 v172, v172\n"
        "v_exp_f32 v173, v173\n"
        "v_exp_f32 v174, v174\n"
        "v_exp_f32 v175, v175\n"
        "v_pk_mul_f32 v[72:73], v[64:65], v[72:73]\n"
        "v_pk_mul_f32 v[74:75], v[66:67], v[74:75]\n"
        "v_pk_add_f32 v[172:173], v[172:173], 1.0 op_sel_hi:[1,0]\n"
        "v_pk_add_f32 v[174:175], v[174:175], 1.0 op_sel_hi:[1,0]\n"
        "v_rcp_f32 v172, v172\n"
        "v_rcp_f32 v173, v173\n"
        "v_rcp_f32 v174, v174\n"
        "v_rcp_f32 v175, v175\n"
        "v_pk_mul_f32 v[72:73], v[72:73], v[172:173]\n"
        "v_pk_mul_f32 v[74:75], v[74:75], v[174:175]\n"
        "v_cvt_pk_bf16_f32 v156, v72, v73\n"
        "v_cvt_pk_bf16_f32 v157, v74, v75\n"
        "v_pk_mul_f32 v[172:173], v[68:69], v[68:69]\n"
        "v_pk_mul_f32 v[174:175], v[70:71], v[70:71]\n"
        "v_pk_fma_f32 v[172:173], v[172:173], s[50:51], v[122:123] op_sel_hi:[1,0,0]\n"
        "v_pk_fma_f32 v[174:175], v[174:175], s[50:51], v[122:123] op_sel_hi:[1,0,0]\n"
        "v_pk_mul_f32 v[172:173], v[172:173], v[68:69]\n"
        "v_pk_mul_f32 v[174:175], v[174:175], v[70:71]\n"
        "v_exp_f32 v172, v172\n"
        "v_exp_f32 v173, v173\n"
        "v_exp_f32 v174, v174\n"
        "v_exp_f32 v175, v175\n"
        "v_pk_mul_f32 v[76:77], v[68:69], v[76:77]\n"
        "v_pk_mul_f32 v[78:79], v[70:71], v[78:79]\n"
        "v_pk_add_f32 v[172:173], v[172:173], 1.0 op_sel_hi:[1,0]\n"
        "s_waitcnt vmcnt(0) lgkmcnt(0)\n"
        "s_barrier\n"
        "ds_read_b128 v[140:143], v124\n"
        "ds_read_b128 v[104:107], v97\n"
        "LATT%=:\n"
        "v_pk_add_f32 v[174:175], v[174:175], 1.0 op_sel_hi:[1,0]\n"
        "v_rcp_f32 v172, v172\n"
        "v_rcp_f32 v173, v173\n"
        "s_waitcnt lgkmcnt(1)\n"
        "v_mfma_f32_32x32x16_bf16 v[176:191], v[140:143], v[132:135], 0\n"
        "v_mfma_f32_32x32x16_bf16 v[192:207], v[140:143], v[128:131], 0\n"
        "v_rcp_f32 v174, v174\n"
        "v_rcp_f32 v175, v175\n"
        "v_pk_mul_f32 v[76:77], v[76:77], v[172:173]\n"
        "v_pk_mul_f32 v[78:79], v[78:79], v[174:175]\n"
        "v_cvt_pk_bf16_f32 v158, v76, v77\n"
        "v_cvt_pk_bf16_f32 v159, v78, v79\n"
        "s_nop 0\n"
        "v_permlane16_swap_b32 v156, v158\n"
        "v_permlane16_swap_b32 v157, v159\n"
        "ds_read_b128 v[108:111], v98\n"
        "ds_read_b128 v[112:115], v98 offset:512\n"
        "s_nop 0\n"
        "v_max3_f32 v208, v176, v177, v178\n"
        "v_max3_f32 v209, v192, v193, v194\n"
        "v_max3_f32 v208, v208, v179, v180\n"
        "v_max3_f32 v209, v209, v195, v196\n"
        "v_max3_f32 v208, v208, v181, v182\n"
        "v_max3_f32 v209, v209, v197, v198\n"
        "v_max3_f32 v208, v208, v183, v184\n"
        "v_max3_f32 v209, v209, v199, v200\n"
        "v_max3_f32 v208, v208, v185, v186\n"
        "v_max3_f32 v209, v209, v201, v202\n"
        "v_max3_f32 v208, v208, v187, v188\n"
        "v_max3_f32 v209, v209, v203, v204\n"
        "v_max3_f32 v208, v208, v189, v190\n"
        "v_max3_f32 v209, v209, v205, v206\n"
        "v_max_f32 v208, v208, v191\n"
        "v_max_f32 v209, v209, v207\n"
        "v_mov_b32 v210, v208\n"
        "v_mov_b32 v211, v209\n"
        "s_nop 0\n"
        "v_permlane32_swap_b32 v208, v210\n"
        "v_permlane32_swap_b32 v209, v211\n"
        "v_min_f32_e64 v32, -v208, -v210\n"
        "v_min_f32_e64 v33, -v208, -v210\n"
        "v_min_f32_e64 v48, -v209, -v211\n"
        "v_min_f32_e64 v49, -v209, -v211\n"
        "v_mov_b64 v[34:35], v[32:33]\n"
        "v_mov_b64 v[36:37], v[32:33]\n"
        "v_mov_b64 v[38:39], v[32:33]\n"
        "v_mov_b64 v[40:41], v[32:33]\n"
        "v_mov_b64 v[42:43], v[32:33]\n"
        "v_mov_b64 v[44:45], v[32:33]\n"
        "v_mov_b64 v[46:47], v[32:33]\n"
        "v_mov_b64 v[50:51], v[48:49]\n"
        "v_mov_b64 v[52:53], v[48:49]\n"
        "s_waitcnt lgkmcnt(0)\n"
        "v_mfma_f32_32x32x16_bf16 v[80:95], v[104:107], v[132:135], v[32:47]\n"
        "v_mov_b64 v[54:55], v[48:49]\n"
        "v_mov_b64 v[56:57], v[48:49]\n"
        "v_mov_b64 v[58:59], v[48:49]\n"
        "v_mov_b64 v[60:61], v[48:49]\n"
        "v_mov_b64 v[62:63], v[48:49]\n"
        "s_add_u32 m0, s54, 32768\n"
        "s_xor_b32 s57, s94, 32768\n"
        "buffer_load_dwordx4 v125, s[28:31], s57 offen sc1 lds\n"
        "s_add_u32 m0, s54, 32768\n"
        "s_xor_b32 s57, s94, 32768\n"
        "buffer_load_dwordx4 v125, s[28:31], s57 offen offset:1024 sc1 lds\n"
        "s_nop 0\n"
        ".p2align 3\n"
        "v_exp_f32 v80, v80\n"
        "v_exp_f32 v81, v81\n"
        "s_setprio 3\n"
        "v_exp_f32 v82, v82\n"
        "ds_read_b128 v[116:119], v97 offset:2048\n"
        "ds_read_b128 v[120:123], v98 offset:2048\n"
        "ds_read_b128 v[140:143], v98 offset:2560\n"
        "v_exp_f32 v83, v83\n"
        "v_exp_f32 v84, v84\n"
        "v_mfma_f32_32x32x16_bf16 v[64:79], v[104:107], v[128:131], v[48:63]\n"
        "v_exp_f32 v85, v85\n"
        "v_exp_f32 v86, v86\n"
        "v_exp_f32 v87, v87\n"
        "v_exp_f32 v88, v88\n"
        "v_exp_f32 v89, v89\n"
        "v_exp_f32 v90, v90\n"
        "v_smfmac_f32_32x32x32_bf16 v[16:31], v[108:111], v[80:87], v96\n"
        "s_cmp_eq_u32 s79, 3\n"
        "s_cbranch_scc1 LX%=\n"
        "buffer_store_dwordx4 v[144:147], v124, s[28:31], s63 offen sc1\n"
        "LX%=:\n"
        "v_exp_f32 v91, v91\n"
        "v_exp_f32 v92, v92\n"
        "v_exp_f32 v93, v93\n"
        "v_exp_f32 v94, v94\n"
        "v_exp_f32 v95, v95\n"
        "v_exp_f32 v64, v64\n"
        "v_exp_f32 v65, v65\n"
        "v_exp_f32 v66, v66\n"
        "v_smfmac_f32_32x32x32_bf16 v[16:31], v[112:115], v[88:95], v96\n"
        "v_exp_f32 v67, v67\n"
        "v_exp_f32 v68, v68\n"
        "s_waitcnt lgkmcnt(0)\n"
        "v_exp_f32 v69, v69\n"
        "v_mfma_f32_32x32x16_bf16 v[80:95], v[116:119], v[132:135], v[32:47]\n"
        "v_exp_f32 v70, v70\n"
        "v_exp_f32 v71, v71\n"
        "v_exp_f32 v72, v72\n"
        "v_exp_f32 v73, v73\n"
        "v_smfmac_f32_32x32x32_bf16 v[0:15], v[108:111], v[64:71], v96\n"
        "s_cmp_eq_u32 s79, 3\n"
        "s_cbranch_scc1 LY%=\n"
        "buffer_store_dwordx4 v[148:151], v124, s[28:31], s51 offen sc1\n"
        "LY%=:\n"
        "v_exp_f32 v74, v74\n"
        "v_exp_f32 v75, v75\n"
        "v_exp_f32 v76, v76\n"
        "v_exp_f32 v77, v77\n"
        "v_exp_f32 v78, v78\n"
        "v_exp_f32 v79, v79\n"
        "s_nop 0\n"
        "v_exp_f32 v80, v80\n"
        "v_exp_f32 v81, v81\n"
        "v_exp_f32 v82, v82\n"
        "v_smfmac_f32_32x32x32_bf16 v[0:15], v[112:115], v[72:79], v96\n"
        "ds_read_b128 v[104:107], v97 offset:4096\n"
        "v_exp_f32 v83, v83\n"
        "s_mov_b64 exec, s[0:1]\n"
        "ds_read_b128 v[108:111], v98 offset:4096\n"
        "ds_read_b128 v[112:115], v98 offset:4608\n"
        "s_mov_b64 exec, -1\n"
        "v_exp_f32 v84, v84\n"
        "v_mfma_f32_32x32x16_bf16 v[64:79], v[116:119], v[128:131], v[48:63]\n"
        "v_exp_f32 v85, v85\n"
        "v_exp_f32 v86, v86\n"
        "v_exp_f32 v87, v87\n"
        "v_exp_f32 v88, v88\n"
        "v_exp_f32 v89, v89\n"
        "v_exp_f32 v90, v90\n"
        "v_smfmac_f32_32x32x32_bf16 v[16:31], v[120:123], v[80:87], v96\n"
        "buffer_store_dwordx4 v[152:155], v124, s[28:31], s64 offen sc1\n"
        "v_exp_f32 v91, v91\n"
        "v_exp_f32 v92, v92\n"
        "v_exp_f32 v93, v93\n"
        "v_exp_f32 v94, v94\n"
        "v_exp_f32 v95, v95\n"
        "v_exp_f32 v64, v64\n"
        "v_exp_f32 v65, v65\n"
        "v_exp_f32 v66, v66\n"
        "v_smfmac_f32_32x32x32_bf16 v[16:31], v[140:143], v[88:95], v96\n"
        "v_exp_f32 v67, v67\n"
        "v_exp_f32 v68, v68\n"
        "s_waitcnt lgkmcnt(0)\n"
        "v_exp_f32 v69, v69\n"
        "v_mfma_f32_32x32x16_bf16 v[80:95], v[104:107], v[132:135], v[32:47]\n"
        "v_exp_f32 v70, v70\n"
        "v_exp_f32 v71, v71\n"
        "v_exp_f32 v72, v72\n"
        "v_exp_f32 v73, v73\n"
        "v_smfmac_f32_32x32x32_bf16 v[0:15], v[120:123], v[64:71], v96\n"
        "buffer_store_dwordx4 v[156:159], v124, s[28:31], s64 offen offset:1024 sc1\n"
        "s_mov_b32 exec_lo, 0\n"
        "s_mov_b32 exec_hi, 0x80000000\n"
        "buffer_store_dword v160, off, s[28:31], s72 sc1\n"
        "s_mov_b64 exec, -1\n"
        "v_exp_f32 v74, v74\n"
        "v_exp_f32 v75, v75\n"
        "v_exp_f32 v76, v76\n"
        "v_exp_f32 v77, v77\n"
        "v_exp_f32 v78, v78\n"
        "v_exp_f32 v79, v79\n"
        "s_nop 0\n"
        "v_exp_f32 v80, v80\n"
        "v_exp_f32 v81, v81\n"
        "v_exp_f32 v82, v82\n"
        "v_smfmac_f32_32x32x32_bf16 v[0:15], v[140:143], v[72:79], v96\n"
        "ds_read_b128 v[116:119], v97 offset:6144\n"
        "v_exp_f32 v83, v83\n"
        "s_mov_b64 exec, s[0:1]\n"
        "ds_read_b128 v[120:123], v98 offset:6144\n"
        "ds_read_b128 v[140:143], v98 offset:6656\n"
        "s_mov_b64 exec, -1\n"
        "v_exp_f32 v84, v84\n"
        "v_mfma_f32_32x32x16_bf16 v[64:79], v[104:107], v[128:131], v[48:63]\n"
        "v_exp_f32 v85, v85\n"
        "v_exp_f32 v86, v86\n"
        "v_exp_f32 v87, v87\n"
        "v_exp_f32 v88, v88\n"
        "v_exp_f32 v89, v89\n"
        "v_exp_f32 v90, v90\n"
        "v_smfmac_f32_32x32x32_bf16 v[16:31], v[108:111], v[80:87], v96\n"
        "s_add_u32 m0, s54, 49152\n"
        "s_xor_b32 s57, s94, 49152\n"
        "buffer_load_dwordx4 v125, s[28:31], s57 offen sc1 lds\n"
        "v_exp_f32 v91, v91\n"
        "v_exp_f32 v92, v92\n"
        "v_exp_f32 v93, v93\n"
        "v_exp_f32 v94, v94\n"
        "v_exp_f32 v95, v95\n"
        "v_exp_f32 v64, v64\n"
        "v_exp_f32 v65, v65\n"
        "v_exp_f32 v66, v66\n"
        "v_smfmac_f32_32x32x32_bf16 v[16:31], v[112:115], v[88:95], v96\n"
        "v_exp_f32 v67, v67\n"
        "v_exp_f32 v68, v68\n"
        "s_waitcnt lgkmcnt(0)\n"
        "v_exp_f32 v69, v69\n"
        "v_mfma_f32_32x32x16_bf16 v[80:95], v[116:119], v[132:135], v[32:47]\n"
        "v_exp_f32 v70, v70\n"
        "v_exp_f32 v71, v71\n"
        "v_exp_f32 v72, v72\n"
        "v_exp_f32 v73, v73\n"
        "v_smfmac_f32_32x32x32_bf16 v[0:15], v[108:111], v[64:71], v96\n"
        "s_add_u32 m0, s54, 49152\n"
        "s_xor_b32 s57, s94, 49152\n"
        "buffer_load_dwordx4 v125, s[28:31], s57 offen offset:1024 sc1 lds\n"
        "v_exp_f32 v74, v74\n"
        "v_exp_f32 v75, v75\n"
        "v_exp_f32 v76, v76\n"
        "v_exp_f32 v77, v77\n"
        "v_exp_f32 v78, v78\n"
        "v_exp_f32 v79, v79\n"
        "s_nop 0\n"
        "v_exp_f32 v80, v80\n"
        "v_exp_f32 v81, v81\n"
        "v_exp_f32 v82, v82\n"
        "v_smfmac_f32_32x32x32_bf16 v[0:15], v[112:115], v[72:79], v96\n"
        "ds_read_b128 v[104:107], v97 offset:16384\n"
        "v_exp_f32 v83, v83\n"
        "s_mov_b64 exec, s[0:1]\n"
        "ds_read_b128 v[108:111], v98 offset:16384\n"
        "ds_read_b128 v[112:115], v98 offset:16896\n"
        "s_mov_b64 exec, -1\n"
        "v_exp_f32 v84, v84\n"
        "v_mfma_f32_32x32x16_bf16 v[64:79], v[116:119], v[128:131], v[48:63]\n"
        "v_exp_f32 v85, v85\n"
        "v_exp_f32 v86, v86\n"
        "v_exp_f32 v87, v87\n"
        "v_exp_f32 v88, v88\n"
        "v_exp_f32 v89, v89\n"
        "v_exp_f32 v90, v90\n"
        "v_smfmac_f32_32x32x32_bf16 v[16:31], v[120:123], v[80:87], v96\n"
        "v_exp_f32 v91, v91\n"
        "v_exp_f32 v92, v92\n"
        "v_exp_f32 v93, v93\n"
        "v_exp_f32 v94, v94\n"
        "v_exp_f32 v95, v95\n"
        "v_exp_f32 v64, v64\n"
        "v_exp_f32 v65, v65\n"
        "v_exp_f32 v66, v66\n"
        "v_smfmac_f32_32x32x32_bf16 v[16:31], v[140:143], v[88:95], v96\n"
        "s_setprio 2\n"
        "v_exp_f32 v67, v67\n"
        "v_exp_f32 v68, v68\n"
        "s_waitcnt lgkmcnt(0)\n"
        "v_mfma_f32_32x32x16_bf16 v[80:95], v[104:107], v[132:135], v[32:47]\n"
        "v_exp_f32 v69, v69\n"
        "v_exp_f32 v70, v70\n"
        "v_exp_f32 v71, v71\n"
        "v_exp_f32 v72, v72\n"
        "v_exp_f32 v73, v73\n"
        "v_exp_f32 v74, v74\n"
        "v_smfmac_f32_32x32x32_bf16 v[0:15], v[120:123], v[64:71], v96\n"
        "v_exp_f32 v75, v75\n"
        "v_exp_f32 v76, v76\n"
        "v_exp_f32 v77, v77\n"
        "v_exp_f32 v78, v78\n"
        "v_exp_f32 v79, v79\n"
        "v_exp_f32 v80, v80\n"
        "v_exp_f32 v81, v81\n"
        "v_exp_f32 v82, v82\n"
        "v_smfmac_f32_32x32x32_bf16 v[0:15], v[140:143], v[72:79], v96\n"
        "ds_read_b128 v[116:119], v97 offset:18432\n"
        "v_exp_f32 v83, v83\n"
        "s_mov_b64 exec, s[0:1]\n"
        "ds_read_b128 v[120:123], v98 offset:18432\n"
        "ds_read_b128 v[140:143], v98 offset:18944\n"
        "s_mov_b64 exec, -1\n"
        "v_exp_f32 v84, v84\n"
        "v_mfma_f32_32x32x16_bf16 v[64:79], v[104:107], v[128:131], v[48:63]\n"
        "v_exp_f32 v85, v85\n"
        "v_exp_f32 v86, v86\n"
        "v_exp_f32 v87, v87\n"
        "v_exp_f32 v88, v88\n"
        "v_exp_f32 v89, v89\n"
        "v_exp_f32 v90, v90\n"
        "v_smfmac_f32_32x32x32_bf16 v[16:31], v[108:111], v[80:87], v96\n"
        "v_exp_f32 v91, v91\n"
        "v_exp_f32 v92, v92\n"
        "v_exp_f32 v93, v93\n"
        "v_exp_f32 v94, v94\n"
        "v_exp_f32 v95, v95\n"
        "v_exp_f32 v64, v64\n"
        "v_exp_f32 v65, v65\n"
        "v_exp_f32 v66, v66\n"
        "v_smfmac_f32_32x32x32_bf16 v[16:31], v[112:115], v[88:95], v96\n"
        "v_exp_f32 v67, v67\n"
        "v_exp_f32 v68, v68\n"
        "s_waitcnt lgkmcnt(0)\n"
        "v_exp_f32 v69, v69\n"
        "v_mfma_f32_32x32x16_bf16 v[80:95], v[116:119], v[132:135], v[32:47]\n"
        "v_exp_f32 v70, v70\n"
        "v_exp_f32 v71, v71\n"
        "v_exp_f32 v72, v72\n"
        "v_exp_f32 v73, v73\n"
        "v_smfmac_f32_32x32x32_bf16 v[0:15], v[108:111], v[64:71], v96\n"
        "s_and_b32 s59, s86, 7\n"
        "s_lshl_b32 s57, s59, 15\n"
        "s_add_u32 s58, s57, s90\n"
        "buffer_load_dword v136, v101, s[20:23], s58 offen\n"
        "v_exp_f32 v74, v74\n"
        "v_exp_f32 v75, v75\n"
        "v_exp_f32 v76, v76\n"
        "v_exp_f32 v77, v77\n"
        "v_exp_f32 v78, v78\n"
        "v_exp_f32 v79, v79\n"
        "s_nop 0\n"
        "v_exp_f32 v80, v80\n"
        "v_exp_f32 v81, v81\n"
        "v_smfmac_f32_32x32x32_bf16 v[0:15], v[112:115], v[72:79], v96\n"
        "ds_read_b128 v[104:107], v97 offset:20480\n"
        "v_exp_f32 v82, v82\n"
        "s_mov_b64 exec, s[0:1]\n"
        "ds_read_b128 v[108:111], v98 offset:20480\n"
        "ds_read_b128 v[112:115], v98 offset:20992\n"
        "s_mov_b64 exec, -1\n"
        "v_exp_f32 v83, v83\n"
        "v_exp_f32 v84, v84\n"
        "v_exp_f32 v85, v85\n"
        "v_mfma_f32_32x32x16_bf16 v[64:79], v[116:119], v[128:131], v[48:63]\n"
        "v_exp_f32 v86, v86\n"
        "v_exp_f32 v87, v87\n"
        "v_exp_f32 v88, v88\n"
        "v_exp_f32 v89, v89\n"
        "v_smfmac_f32_32x32x32_bf16 v[16:31], v[120:123], v[80:87], v96\n"
        "s_add_u32 s58, s58, 0x1000\n"
        "buffer_load_dword v136, v101, s[20:23], s58 offen\n"
        "v_exp_f32 v90, v90\n"
        "v_exp_f32 v91, v91\n"
        "v_exp_f32 v92, v92\n"
        "v_exp_f32 v93, v93\n"
        "v_exp_f32 v94, v94\n"
        "v_exp_f32 v95, v95\n"
        "s_nop 0\n"
        "v_exp_f32 v64, v64\n"
        "v_exp_f32 v65, v65\n"
        "v_exp_f32 v66, v66\n"
        "v_smfmac_f32_32x32x32_bf16 v[16:31], v[140:143], v[88:95], v96\n"
        "s_setprio 1\n"
        "v_exp_f32 v67, v67\n"
        "v_exp_f32 v68, v68\n"
        "s_waitcnt lgkmcnt(0)\n"
        "v_mfma_f32_32x32x16_bf16 v[80:95], v[104:107], v[132:135], v[32:47]\n"
        "v_exp_f32 v69, v69\n"
        "v_exp_f32 v70, v70\n"
        "v_exp_f32 v71, v71\n"
        "v_exp_f32 v72, v72\n"
        "v_exp_f32 v73, v73\n"
        "v_exp_f32 v74, v74\n"
        "v_smfmac_f32_32x32x32_bf16 v[0:15], v[120:123], v[64:71], v96\n"
        "s_add_u32 s59, s59, 8\n"
        "s_min_u32 s59, s59, 13\n"
        "s_lshl_b32 s57, s59, 15\n"
        "s_add_u32 s58, s57, s90\n"
        "buffer_load_dword v136, v101, s[20:23], s58 offen\n"
        "v_exp_f32 v75, v75\n"
        "v_exp_f32 v76, v76\n"
        "v_exp_f32 v77, v77\n"
        "v_exp_f32 v78, v78\n"
        "v_exp_f32 v79, v79\n"
        "v_exp_f32 v80, v80\n"
        "v_exp_f32 v81, v81\n"
        "v_exp_f32 v82, v82\n"
        "v_smfmac_f32_32x32x32_bf16 v[0:15], v[140:143], v[72:79], v96\n"
        "ds_read_b128 v[116:119], v97 offset:22528\n"
        "v_exp_f32 v83, v83\n"
        "s_mov_b64 exec, s[0:1]\n"
        "ds_read_b128 v[120:123], v98 offset:22528\n"
        "ds_read_b128 v[140:143], v98 offset:23040\n"
        "s_mov_b64 exec, -1\n"
        "v_exp_f32 v84, v84\n"
        "v_mfma_f32_32x32x16_bf16 v[64:79], v[104:107], v[128:131], v[48:63]\n"
        "v_exp_f32 v85, v85\n"
        "v_exp_f32 v86, v86\n"
        "v_exp_f32 v87, v87\n"
        "v_exp_f32 v88, v88\n"
        "v_exp_f32 v89, v89\n"
        "v_exp_f32 v90, v90\n"
        "v_smfmac_f32_32x32x32_bf16 v[16:31], v[108:111], v[80:87], v96\n"
        "s_add_u32 s58, s58, 0x1000\n"
        "buffer_load_dword v136, v101, s[20:23], s58 offen\n"
        "v_exp_f32 v91, v91\n"
        "v_exp_f32 v92, v92\n"
        "v_exp_f32 v93, v93\n"
        "v_exp_f32 v94, v94\n"
        "v_exp_f32 v95, v95\n"
        "v_exp_f32 v64, v64\n"
        "v_exp_f32 v65, v65\n"
        "v_exp_f32 v66, v66\n"
        "v_smfmac_f32_32x32x32_bf16 v[16:31], v[112:115], v[88:95], v96\n"
        "s_setprio 0\n"
        "v_exp_f32 v67, v67\n"
        "v_exp_f32 v68, v68\n"
        "s_waitcnt lgkmcnt(0)\n"
        "v_mfma_f32_32x32x16_bf16 v[80:95], v[116:119], v[132:135], v[32:47]\n"
        "v_exp_f32 v69, v69\n"
        "v_exp_f32 v70, v70\n"
        "v_exp_f32 v71, v71\n"
        "v_exp_f32 v72, v72\n"
        "v_exp_f32 v73, v73\n"
        "v_exp_f32 v74, v74\n"
        "v_smfmac_f32_32x32x32_bf16 v[0:15], v[108:111], v[64:71], v96\n"
        "s_and_b32 s57, s86, 6\n"
        "s_lshl_b32 s57, s57, 14\n"
        "s_and_b32 s58, s86, 1\n"
        "s_lshl_b32 s58, s58, 12\n"
        "s_add_u32 s57, s57, s58\n"
        "s_add_u32 s57, s57, s91\n"
        "buffer_load_dword v136, v101, s[24:27], s57 offen\n"
        "v_exp_f32 v75, v75\n"
        "v_exp_f32 v76, v76\n"
        "v_exp_f32 v77, v77\n"
        "v_exp_f32 v78, v78\n"
        "v_exp_f32 v79, v79\n"
        "v_exp_f32 v80, v80\n"
        "v_exp_f32 v81, v81\n"
        "v_exp_f32 v82, v82\n"
        "v_smfmac_f32_32x32x32_bf16 v[0:15], v[112:115], v[72:79], v96\n"
        "s_waitcnt vmcnt(5) lgkmcnt(0)\n"
        "s_barrier\n"
        "s_cmp_lg_u32 s52, 0\n"
        "s_cbranch_scc1 LG%=\n"
        "s_mov_b64 exec, 1\n"
        "v_mov_b32 v138, s48\n"
        "v_mov_b32 v139, s49\n"
        "buffer_store_dwordx2 v[138:139], off, s[28:31], s67 sc1\n"
        "s_mov_b64 exec, -1\n"
        "LG%=:\n"
        "s_setprio 3\n"
        "v_exp_f32 v83, v83\n"
        "ds_read_b128 v[104:107], v97 offset:32768\n"
        "v_exp_f32 v84, v84\n"
        "s_mov_b64 exec, s[0:1]\n"
        "ds_read_b128 v[108:111], v98 offset:32768\n"
        "ds_read_b128 v[112:115], v98 offset:33280\n"
        "s_mov_b64 exec, -1\n"
        "v_exp_f32 v85, v85\n"
        "v_mfma_f32_32x32x16_bf16 v[64:79], v[116:119], v[128:131], v[48:63]\n"
        "v_exp_f32 v86, v86\n"
        "v_exp_f32 v87, v87\n"
        "v_exp_f32 v88, v88\n"
        "v_exp_f32 v89, v89\n"
        "v_smfmac_f32_32x32x32_bf16 v[16:31], v[120:123], v[80:87], v96\n"
        "s_mov_b32 m0, s54\n"
        "s_xor_b32 s57, s94, 65536\n"
        "buffer_load_dwordx4 v125, s[28:31], s57 offen sc1 lds\n"
        "v_exp_f32 v90, v90\n"
        "v_exp_f32 v91, v91\n"
        "v_exp_f32 v92, v92\n"
        "v_exp_f32 v93, v93\n"
        "v_exp_f32 v94, v94\n"
        "v_exp_f32 v95, v95\n"
        "s_nop 0\n"
        "v_exp_f32 v64, v64\n"
        "v_exp_f32 v65, v65\n"
        "v_smfmac_f32_32x32x32_bf16 v[16:31], v[140:143], v[88:95], v96\n"
        "v_exp_f32 v66, v66\n"
        "v_exp_f32 v67, v67\n"
        "v_exp_f32 v68, v68\n"
        "s_waitcnt lgkmcnt(0)\n"
        "v_mfma_f32_32x32x16_bf16 v[80:95], v[104:107], v[132:135], v[32:47]\n"
        "v_exp_f32 v69, v69\n"
        "v_exp_f32 v70, v70\n"
        "v_exp_f32 v71, v71\n"
        "v_exp_f32 v72, v72\n"
        "v_exp_f32 v73, v73\n"
        "v_exp_f32 v74, v74\n"
        "v_smfmac_f32_32x32x32_bf16 v[0:15], v[120:123], v[64:71], v96\n"
        "s_mov_b32 m0, s54\n"
        "s_xor_b32 s57, s94, 65536\n"
        "buffer_load_dwordx4 v125, s[28:31], s57 offen offset:1024 sc1 lds\n"
        "v_exp_f32 v75, v75\n"
        "v_exp_f32 v76, v76\n"
        "v_exp_f32 v77, v77\n"
        "v_exp_f32 v78, v78\n"
        "v_exp_f32 v79, v79\n"
        "v_exp_f32 v80, v80\n"
        "v_exp_f32 v81, v81\n"
        "v_smfmac_f32_32x32x32_bf16 v[0:15], v[140:143], v[72:79], v96\n"
        "ds_read_b128 v[116:119], v97 offset:34816\n"
        "v_exp_f32 v82, v82\n"
        "s_mov_b64 exec, s[0:1]\n"
        "ds_read_b128 v[120:123], v98 offset:34816\n"
        "ds_read_b128 v[140:143], v98 offset:35328\n"
        "s_mov_b64 exec, -1\n"
        "v_exp_f32 v83, v83\n"
        "v_exp_f32 v84, v84\n"
        "v_exp_f32 v85, v85\n"
        "v_mfma_f32_32x32x16_bf16 v[64:79], v[104:107], v[128:131], v[48:63]\n"
        "v_exp_f32 v86, v86\n"
        "v_exp_f32 v87, v87\n"
        "v_exp_f32 v88, v88\n"
        "v_exp_f32 v89, v89\n"
        "v_smfmac_f32_32x32x32_bf16 v[16:31], v[108:111], v[80:87], v96\n"
        "s_add_u32 m0, s54, 16384\n"
        "s_xor_b32 s57, s94, 81920\n"
        "buffer_load_dwordx4 v125, s[28:31], s57 offen sc1 lds\n"
        "v_exp_f32 v90, v90\n"
        "v_exp_f32 v91, v91\n"
        "v_exp_f32 v92, v92\n"
        "v_exp_f32 v93, v93\n"
        "v_exp_f32 v94, v94\n"
        "v_exp_f32 v95, v95\n"
        "s_nop 0\n"
        "v_exp_f32 v64, v64\n"
        "v_exp_f32 v65, v65\n"
        "v_exp_f32 v66, v66\n"
        "v_smfmac_f32_32x32x32_bf16 v[16:31], v[112:115], v[88:95], v96\n"
        "v_exp_f32 v67, v67\n"
        "v_exp_f32 v68, v68\n"
        "s_waitcnt lgkmcnt(0)\n"
        "v_exp_f32 v69, v69\n"
        "v_mfma_f32_32x32x16_bf16 v[80:95], v[116:119], v[132:135], v[32:47]\n"
        "v_exp_f32 v70, v70\n"
        "v_exp_f32 v71, v71\n"
        "v_exp_f32 v72, v72\n"
        "v_exp_f32 v73, v73\n"
        "v_smfmac_f32_32x32x32_bf16 v[0:15], v[108:111], v[64:71], v96\n"
        "s_add_u32 m0, s54, 16384\n"
        "s_xor_b32 s57, s94, 81920\n"
        "buffer_load_dwordx4 v125, s[28:31], s57 offen offset:1024 sc1 lds\n"
        "v_exp_f32 v74, v74\n"
        "v_exp_f32 v75, v75\n"
        "v_exp_f32 v76, v76\n"
        "v_exp_f32 v77, v77\n"
        "v_exp_f32 v78, v78\n"
        "v_exp_f32 v79, v79\n"
        "s_nop 0\n"
        "v_exp_f32 v80, v80\n"
        "v_exp_f32 v81, v81\n"
        "v_exp_f32 v82, v82\n"
        "v_smfmac_f32_32x32x32_bf16 v[0:15], v[112:115], v[72:79], v96\n"
        "ds_read_b128 v[104:107], v97 offset:36864\n"
        "v_exp_f32 v83, v83\n"
        "s_mov_b64 exec, s[0:1]\n"
        "ds_read_b128 v[108:111], v98 offset:36864\n"
        "ds_read_b128 v[112:115], v98 offset:37376\n"
        "s_mov_b64 exec, -1\n"
        "v_exp_f32 v84, v84\n"
        "v_mfma_f32_32x32x16_bf16 v[64:79], v[116:119], v[128:131], v[48:63]\n"
        "v_exp_f32 v85, v85\n"
        "v_exp_f32 v86, v86\n"
        "v_exp_f32 v87, v87\n"
        "v_exp_f32 v88, v88\n"
        "v_exp_f32 v89, v89\n"
        "v_exp_f32 v90, v90\n"
        "v_smfmac_f32_32x32x32_bf16 v[16:31], v[120:123], v[80:87], v96\n"
        "s_add_u32 m0, s54, 81920\n"
        "s_add_u32 s57, s90, 0\n"
        "buffer_load_dwordx4 v101, s[20:23], s57 offen lds\n"
        "v_exp_f32 v91, v91\n"
        "v_exp_f32 v92, v92\n"
        "v_exp_f32 v93, v93\n"
        "v_exp_f32 v94, v94\n"
        "v_exp_f32 v95, v95\n"
        "v_exp_f32 v64, v64\n"
        "v_exp_f32 v65, v65\n"
        "v_smfmac_f32_32x32x32_bf16 v[16:31], v[140:143], v[88:95], v96\n"
        "v_exp_f32 v66, v66\n"
        "v_exp_f32 v67, v67\n"
        "v_exp_f32 v68, v68\n"
        "s_waitcnt lgkmcnt(0)\n"
        "v_mfma_f32_32x32x16_bf16 v[80:95], v[104:107], v[132:135], v[32:47]\n"
        "v_exp_f32 v69, v69\n"
        "v_exp_f32 v70, v70\n"
        "v_exp_f32 v71, v71\n"
        "v_exp_f32 v72, v72\n"
        "v_exp_f32 v73, v73\n"
        "v_exp_f32 v74, v74\n"
        "v_smfmac_f32_32x32x32_bf16 v[0:15], v[120:123], v[64:71], v96\n"
        "s_add_u32 m0, s54, 82944\n"
        "s_add_u32 s57, s90, 4096\n"
        "buffer_load_dwordx4 v101, s[20:23], s57 offen lds\n"
        "v_exp_f32 v75, v75\n"
        "v_exp_f32 v76, v76\n"
        "v_exp_f32 v77, v77\n"
        "v_exp_f32 v78, v78\n"
        "v_exp_f32 v79, v79\n"
        "v_exp_f32 v80, v80\n"
        "v_exp_f32 v81, v81\n"
        "v_exp_f32 v82, v82\n"
        "v_smfmac_f32_32x32x32_bf16 v[0:15], v[140:143], v[72:79], v96\n"
        "ds_read_b128 v[116:119], v97 offset:38912\n"
        "v_exp_f32 v83, v83\n"
        "s_mov_b64 exec, s[0:1]\n"
        "ds_read_b128 v[120:123], v98 offset:38912\n"
        "ds_read_b128 v[140:143], v98 offset:39424\n"
        "s_mov_b64 exec, -1\n"
        "v_exp_f32 v84, v84\n"
        "v_mfma_f32_32x32x16_bf16 v[64:79], v[104:107], v[128:131], v[48:63]\n"
        "v_exp_f32 v85, v85\n"
        "v_exp_f32 v86, v86\n"
        "v_exp_f32 v87, v87\n"
        "v_exp_f32 v88, v88\n"
        "v_exp_f32 v89, v89\n"
        "v_exp_f32 v90, v90\n"
        "v_smfmac_f32_32x32x32_bf16 v[16:31], v[108:111], v[80:87], v96\n"
        "s_add_u32 m0, s54, 98304\n"
        "s_add_u32 s57, s90, 32768\n"
        "buffer_load_dwordx4 v101, s[20:23], s57 offen lds\n"
        "v_exp_f32 v91, v91\n"
        "v_exp_f32 v92, v92\n"
        "v_exp_f32 v93, v93\n"
        "v_exp_f32 v94, v94\n"
        "v_exp_f32 v95, v95\n"
        "v_exp_f32 v64, v64\n"
        "v_exp_f32 v65, v65\n"
        "v_exp_f32 v66, v66\n"
        "v_smfmac_f32_32x32x32_bf16 v[16:31], v[112:115], v[88:95], v96\n"
        "v_exp_f32 v67, v67\n"
        "v_exp_f32 v68, v68\n"
        "s_waitcnt lgkmcnt(0)\n"
        "v_exp_f32 v69, v69\n"
        "v_mfma_f32_32x32x16_bf16 v[80:95], v[116:119], v[132:135], v[32:47]\n"
        "v_exp_f32 v70, v70\n"
        "v_exp_f32 v71, v71\n"
        "v_exp_f32 v72, v72\n"
        "v_exp_f32 v73, v73\n"
        "v_smfmac_f32_32x32x32_bf16 v[0:15], v[108:111], v[64:71], v96\n"
        "buffer_load_dwordx2 v[138:139], v102, s[28:31], 0 offen sc1\n"
        "v_exp_f32 v74, v74\n"
        "v_exp_f32 v75, v75\n"
        "v_exp_f32 v76, v76\n"
        "v_exp_f32 v77, v77\n"
        "v_exp_f32 v78, v78\n"
        "v_exp_f32 v79, v79\n"
        "s_nop 0\n"
        "v_exp_f32 v80, v80\n"
        "v_exp_f32 v81, v81\n"
        "v_exp_f32 v82, v82\n"
        "v_smfmac_f32_32x32x32_bf16 v[0:15], v[112:115], v[72:79], v96\n"
        "ds_read_b128 v[104:107], v97 offset:49152\n"
        "v_exp_f32 v83, v83\n"
        "s_mov_b64 exec, s[0:1]\n"
        "ds_read_b128 v[108:111], v98 offset:49152\n"
        "ds_read_b128 v[112:115], v98 offset:49664\n"
        "s_mov_b64 exec, -1\n"
        "v_exp_f32 v84, v84\n"
        "v_mfma_f32_32x32x16_bf16 v[64:79], v[116:119], v[128:131], v[48:63]\n"
        "v_exp_f32 v85, v85\n"
        "v_exp_f32 v86, v86\n"
        "v_exp_f32 v87, v87\n"
        "v_exp_f32 v88, v88\n"
        "v_exp_f32 v89, v89\n"
        "v_exp_f32 v90, v90\n"
        "v_smfmac_f32_32x32x32_bf16 v[16:31], v[120:123], v[80:87], v96\n"
        "s_add_u32 m0, s54, 99328\n"
        "s_add_u32 s57, s90, 36864\n"
        "buffer_load_dwordx4 v101, s[20:23], s57 offen lds\n"
        "v_exp_f32 v91, v91\n"
        "v_exp_f32 v92, v92\n"
        "v_exp_f32 v93, v93\n"
        "v_exp_f32 v94, v94\n"
        "v_exp_f32 v95, v95\n"
        "v_exp_f32 v64, v64\n"
        "v_exp_f32 v65, v65\n"
        "v_exp_f32 v66, v66\n"
        "v_smfmac_f32_32x32x32_bf16 v[16:31], v[140:143], v[88:95], v96\n"
        "s_setprio 2\n"
        "v_exp_f32 v67, v67\n"
        "v_exp_f32 v68, v68\n"
        "s_waitcnt lgkmcnt(0)\n"
        "v_mfma_f32_32x32x16_bf16 v[80:95], v[104:107], v[132:135], v[32:47]\n"
        "v_exp_f32 v69, v69\n"
        "v_exp_f32 v70, v70\n"
        "v_exp_f32 v71, v71\n"
        "v_exp_f32 v72, v72\n"
        "v_exp_f32 v73, v73\n"
        "v_exp_f32 v74, v74\n"
        "v_smfmac_f32_32x32x32_bf16 v[0:15], v[120:123], v[64:71], v96\n"
        "s_add_u32 m0, s54, 114688\n"
        "s_add_u32 s57, s90, 65536\n"
        "buffer_load_dwordx4 v101, s[20:23], s57 offen lds\n"
        "v_exp_f32 v75, v75\n"
        "v_exp_f32 v76, v76\n"
        "v_exp_f32 v77, v77\n"
        "v_exp_f32 v78, v78\n"
        "v_exp_f32 v79, v79\n"
        "v_exp_f32 v80, v80\n"
        "v_exp_f32 v81, v81\n"
        "v_exp_f32 v82, v82\n"
        "v_smfmac_f32_32x32x32_bf16 v[0:15], v[140:143], v[72:79], v96\n"
        "ds_read_b128 v[116:119], v97 offset:51200\n"
        "v_exp_f32 v83, v83\n"
        "s_mov_b64 exec, s[0:1]\n"
        "ds_read_b128 v[120:123], v98 offset:51200\n"
        "ds_read_b128 v[140:143], v98 offset:51712\n"
        "s_mov_b64 exec, -1\n"
        "v_exp_f32 v84, v84\n"
        "v_mfma_f32_32x32x16_bf16 v[64:79], v[104:107], v[128:131], v[48:63]\n"
        "v_exp_f32 v85, v85\n"
        "v_exp_f32 v86, v86\n"
        "v_exp_f32 v87, v87\n"
        "v_exp_f32 v88, v88\n"
        "v_exp_f32 v89, v89\n"
        "v_exp_f32 v90, v90\n"
        "v_smfmac_f32_32x32x32_bf16 v[16:31], v[108:111], v[80:87], v96\n"
        "s_waitcnt vmcnt(2)\n"
        "v_cmp_ne_u64_e64 vcc, v[138:139], s[48:49]\n"
        "s_mov_b32 s77, 0x100000\n"
        "s_cmp_eq_u64 vcc, 0\n"
        "s_cbranch_scc1 LHQ%=\n"
        "LHP%=:\n"
        "s_sleep 1\n"
        "buffer_load_dwordx2 v[138:139], v102, s[28:31], 0 offen sc1\n"
        "s_sub_u32 s77, s77, 1\n"
        "s_cmp_eq_u32 s77, 0\n"
        "s_waitcnt vmcnt(0)\n"
        "s_cbranch_scc1 LHQ%=\n"
        "v_cmp_ne_u64_e64 vcc, v[138:139], s[48:49]\n"
        "s_nop 0\n"
        "s_cmp_eq_u64 vcc, 0\n"
        "s_cbranch_scc0 LHP%=\n"
        "LHQ%=:\n"
        "buffer_load_dword v137, v103, s[28:31], 0 offen sc1\n"
        "v_exp_f32 v91, v91\n"
        "v_exp_f32 v92, v92\n"
        "v_exp_f32 v93, v93\n"
        "v_exp_f32 v94, v94\n"
        "v_exp_f32 v95, v95\n"
        "v_exp_f32 v64, v64\n"
        "v_exp_f32 v65, v65\n"
        "v_smfmac_f32_32x32x32_bf16 v[16:31], v[112:115], v[88:95], v96\n"
        "v_exp_f32 v66, v66\n"
        "v_exp_f32 v67, v67\n"
        "v_exp_f32 v68, v68\n"
        "s_waitcnt lgkmcnt(0)\n"
        "v_mfma_f32_32x32x16_bf16 v[80:95], v[116:119], v[132:135], v[32:47]\n"
        "v_exp_f32 v69, v69\n"
        "v_exp_f32 v70, v70\n"
        "v_exp_f32 v71, v71\n"
        "v_exp_f32 v72, v72\n"
        "v_exp_f32 v73, v73\n"
        "v_exp_f32 v74, v74\n"
        "v_smfmac_f32_32x32x32_bf16 v[0:15], v[108:111], v[64:71], v96\n"
        "buffer_load_dwordx4 v[144:147], v100, s[28:31], s74 offen sc1\n"
        "v_exp_f32 v75, v75\n"
        "v_exp_f32 v76, v76\n"
        "v_exp_f32 v77, v77\n"
        "v_exp_f32 v78, v78\n"
        "v_exp_f32 v79, v79\n"
        "v_exp_f32 v80, v80\n"
        "v_exp_f32 v81, v81\n"
        "v_exp_f32 v82, v82\n"
        "v_smfmac_f32_32x32x32_bf16 v[0:15], v[112:115], v[72:79], v96\n"
        "ds_read_b128 v[104:107], v97 offset:53248\n"
        "v_exp_f32 v83, v83\n"
        "s_mov_b64 exec, s[0:1]\n"
        "ds_read_b128 v[108:111], v98 offset:53248\n"
        "ds_read_b128 v[112:115], v98 offset:53760\n"
        "s_mov_b64 exec, -1\n"
        "v_exp_f32 v84, v84\n"
        "v_mfma_f32_32x32x16_bf16 v[64:79], v[116:119], v[128:131], v[48:63]\n"
        "v_exp_f32 v85, v85\n"
        "v_exp_f32 v86, v86\n"
        "v_exp_f32 v87, v87\n"
        "v_exp_f32 v88, v88\n"
        "v_exp_f32 v89, v89\n"
        "v_exp_f32 v90, v90\n"
        "v_smfmac_f32_32x32x32_bf16 v[16:31], v[120:123], v[80:87], v96\n"
        "buffer_load_dwordx4 v[148:151], v100, s[28:31], s74 offen offset:256 sc1\n"
        "v_exp_f32 v91, v91\n"
        "v_exp_f32 v92, v92\n"
        "v_exp_f32 v93, v93\n"
        "v_exp_f32 v94, v94\n"
        "v_exp_f32 v95, v95\n"
        "v_exp_f32 v64, v64\n"
        "v_exp_f32 v65, v65\n"
        "v_exp_f32 v66, v66\n"
        "v_smfmac_f32_32x32x32_bf16 v[16:31], v[140:143], v[88:95], v96\n"
        "s_setprio 1\n"
        "v_exp_f32 v67, v67\n"
        "v_exp_f32 v68, v68\n"
        "s_waitcnt lgkmcnt(0)\n"
        "v_mfma_f32_32x32x16_bf16 v[80:95], v[104:107], v[132:135], v[32:47]\n"
        "v_exp_f32 v69, v69\n"
        "v_exp_f32 v70, v70\n"
        "v_exp_f32 v71, v71\n"
        "v_exp_f32 v72, v72\n"
        "v_exp_f32 v73, v73\n"
        "v_exp_f32 v74, v74\n"
        "v_smfmac_f32_32x32x32_bf16 v[0:15], v[120:123], v[64:71], v96\n"
        "buffer_load_dwordx4 v[152:155], v100, s[28:31], s74 offen offset:2048 sc1\n"
        "v_exp_f32 v75, v75\n"
        "v_exp_f32 v76, v76\n"
        "v_exp_f32 v77, v77\n"
        "v_exp_f32 v78, v78\n"
        "v_exp_f32 v79, v79\n"
        "v_exp_f32 v80, v80\n"
        "v_exp_f32 v81, v81\n"
        "v_exp_f32 v82, v82\n"
        "v_smfmac_f32_32x32x32_bf16 v[0:15], v[140:143], v[72:79], v96\n"
        "ds_read_b128 v[116:119], v97 offset:55296\n"
        "v_exp_f32 v83, v83\n"
        "s_mov_b64 exec, s[0:1]\n"
        "ds_read_b128 v[120:123], v98 offset:55296\n"
        "ds_read_b128 v[140:143], v98 offset:55808\n"
        "s_mov_b64 exec, -1\n"
        "v_exp_f32 v84, v84\n"
        "v_mfma_f32_32x32x16_bf16 v[64:79], v[104:107], v[128:131], v[48:63]\n"
        "v_exp_f32 v85, v85\n"
        "v_exp_f32 v86, v86\n"
        "v_exp_f32 v87, v87\n"
        "v_exp_f32 v88, v88\n"
        "v_exp_f32 v89, v89\n"
        "v_exp_f32 v90, v90\n"
        "v_smfmac_f32_32x32x32_bf16 v[16:31], v[108:111], v[80:87], v96\n"
        "buffer_load_dwordx4 v[156:159], v100, s[28:31], s74 offen offset:2304 sc1\n"
        "v_exp_f32 v91, v91\n"
        "v_exp_f32 v92, v92\n"
        "v_exp_f32 v93, v93\n"
        "v_exp_f32 v94, v94\n"
        "v_exp_f32 v95, v95\n"
        "v_exp_f32 v64, v64\n"
        "v_exp_f32 v65, v65\n"
        "v_exp_f32 v66, v66\n"
        "v_smfmac_f32_32x32x32_bf16 v[16:31], v[112:115], v[88:95], v96\n"
        "s_setprio 0\n"
        "v_exp_f32 v67, v67\n"
        "v_exp_f32 v68, v68\n"
        "s_waitcnt lgkmcnt(0)\n"
        "v_mfma_f32_32x32x16_bf16 v[80:95], v[116:119], v[132:135], v[32:47]\n"
        "v_exp_f32 v69, v69\n"
        "v_exp_f32 v70, v70\n"
        "v_exp_f32 v71, v71\n"
        "v_exp_f32 v72, v72\n"
        "v_exp_f32 v73, v73\n"
        "v_exp_f32 v74, v74\n"
        "v_smfmac_f32_32x32x32_bf16 v[0:15], v[108:111], v[64:71], v96\n"
        "s_add_u32 s74, s74, 0x1000\n"
        "buffer_load_dwordx4 v[160:163], v100, s[28:31], s74 offen sc1\n"
        "v_exp_f32 v75, v75\n"
        "v_exp_f32 v76, v76\n"
        "v_exp_f32 v77, v77\n"
        "v_exp_f32 v78, v78\n"
        "v_exp_f32 v79, v79\n"
        "v_exp_f32 v80, v80\n"
        "v_exp_f32 v81, v81\n"
        "v_exp_f32 v82, v82\n"
        "v_smfmac_f32_32x32x32_bf16 v[0:15], v[112:115], v[72:79], v96\n"
        "s_waitcnt vmcnt(8) lgkmcnt(0)\n"
        "s_barrier\n"
        "s_setprio 3\n"
        "v_exp_f32 v83, v83\n"
        "ds_read_b128 v[104:107], v97 offset:0\n"
        "v_exp_f32 v84, v84\n"
        "s_mov_b64 exec, s[0:1]\n"
        "ds_read_b128 v[108:111], v98 offset:0\n"
        "ds_read_b128 v[112:115], v98 offset:512\n"
        "s_mov_b64 exec, -1\n"
        "v_exp_f32 v85, v85\n"
        "v_mfma_f32_32x32x16_bf16 v[64:79], v[116:119], v[128:131], v[48:63]\n"
        "v_exp_f32 v86, v86\n"
        "v_exp_f32 v87, v87\n"
        "v_exp_f32 v88, v88\n"
        "v_exp_f32 v89, v89\n"
        "v_smfmac_f32_32x32x32_bf16 v[16:31], v[120:123], v[80:87], v96\n"
        "s_add_u32 m0, s54, 32768\n"
        "s_xor_b32 s57, s94, 98304\n"
        "buffer_load_dwordx4 v125, s[28:31], s57 offen sc1 lds\n"
        "v_exp_f32 v90, v90\n"
        "v_exp_f32 v91, v91\n"
        "v_exp_f32 v92, v92\n"
        "v_exp_f32 v93, v93\n"
        "v_exp_f32 v94, v94\n"
        "v_exp_f32 v95, v95\n"
        "s_nop 0\n"
        "v_exp_f32 v64, v64\n"
        "v_exp_f32 v65, v65\n"
        "v_exp_f32 v66, v66\n"
        "v_smfmac_f32_32x32x32_bf16 v[16:31], v[140:143], v[88:95], v96\n"
        "v_exp_f32 v67, v67\n"
        "v_exp_f32 v68, v68\n"
        "s_waitcnt lgkmcnt(0)\n"
        "v_exp_f32 v69, v69\n"
        "v_mfma_f32_32x32x16_bf16 v[80:95], v[104:107], v[132:135], v[32:47]\n"
        "v_exp_f32 v70, v70\n"
        "v_exp_f32 v71, v71\n"
        "v_exp_f32 v72, v72\n"
        "v_exp_f32 v73, v73\n"
        "v_smfmac_f32_32x32x32_bf16 v[0:15], v[120:123], v[64:71], v96\n"
        "s_add_u32 m0, s54, 32768\n"
        "s_xor_b32 s57, s94, 98304\n"
        "buffer_load_dwordx4 v125, s[28:31], s57 offen offset:1024 sc1 lds\n"
        "v_exp_f32 v74, v74\n"
        "v_exp_f32 v75, v75\n"
        "v_exp_f32 v76, v76\n"
        "v_exp_f32 v77, v77\n"
        "v_exp_f32 v78, v78\n"
        "v_exp_f32 v79, v79\n"
        "s_nop 0\n"
        "v_exp_f32 v80, v80\n"
        "v_exp_f32 v81, v81\n"
        "v_exp_f32 v82, v82\n"
        "v_smfmac_f32_32x32x32_bf16 v[0:15], v[140:143], v[72:79], v96\n"
        "ds_read_b128 v[116:119], v97 offset:2048\n"
        "v_exp_f32 v83, v83\n"
        "s_mov_b64 exec, s[0:1]\n"
        "ds_read_b128 v[120:123], v98 offset:2048\n"
        "ds_read_b128 v[140:143], v98 offset:2560\n"
        "s_mov_b64 exec, -1\n"
        "v_exp_f32 v84, v84\n"
        "v_mfma_f32_32x32x16_bf16 v[64:79], v[104:107], v[128:131], v[48:63]\n"
        "v_exp_f32 v85, v85\n"
        "v_exp_f32 v86, v86\n"
        "v_exp_f32 v87, v87\n"
        "v_exp_f32 v88, v88\n"
        "v_exp_f32 v89, v89\n"
        "v_exp_f32 v90, v90\n"
        "v_smfmac_f32_32x32x32_bf16 v[16:31], v[108:111], v[80:87], v96\n"
        "s_add_u32 m0, s54, 49152\n"
        "s_xor_b32 s57, s94, 114688\n"
        "buffer_load_dwordx4 v125, s[28:31], s57 offen sc1 lds\n"
        "v_exp_f32 v91, v91\n"
        "v_exp_f32 v92, v92\n"
        "v_exp_f32 v93, v93\n"
        "v_exp_f32 v94, v94\n"
        "v_exp_f32 v95, v95\n"
        "v_exp_f32 v64, v64\n"
        "v_exp_f32 v65, v65\n"
        "v_exp_f32 v66, v66\n"
        "v_smfmac_f32_32x32x32_bf16 v[16:31], v[112:115], v[88:95], v96\n"
        "v_exp_f32 v67, v67\n"
        "v_exp_f32 v68, v68\n"
        "s_waitcnt lgkmcnt(0)\n"
        "v_exp_f32 v69, v69\n"
        "v_mfma_f32_32x32x16_bf16 v[80:95], v[116:119], v[132:135], v[32:47]\n"
        "v_exp_f32 v70, v70\n"
        "v_exp_f32 v71, v71\n"
        "v_exp_f32 v72, v72\n"
        "v_exp_f32 v73, v73\n"
        "v_smfmac_f32_32x32x32_bf16 v[0:15], v[108:111], v[64:71], v96\n"
        "s_add_u32 m0, s54, 49152\n"
        "s_xor_b32 s57, s94, 114688\n"
        "buffer_load_dwordx4 v125, s[28:31], s57 offen offset:1024 sc1 lds\n"
        "v_exp_f32 v74, v74\n"
        "v_exp_f32 v75, v75\n"
        "v_exp_f32 v76, v76\n"
        "v_exp_f32 v77, v77\n"
        "v_exp_f32 v78, v78\n"
        "v_exp_f32 v79, v79\n"
        "s_nop 0\n"
        "v_exp_f32 v80, v80\n"
        "v_exp_f32 v81, v81\n"
        "v_exp_f32 v82, v82\n"
        "v_smfmac_f32_32x32x32_bf16 v[0:15], v[112:115], v[72:79], v96\n"
        "ds_read_b128 v[104:107], v97 offset:4096\n"
        "v_exp_f32 v83, v83\n"
        "s_mov_b64 exec, s[0:1]\n"
        "ds_read_b128 v[108:111], v98 offset:4096\n"
        "ds_read_b128 v[112:115], v98 offset:4608\n"
        "s_mov_b64 exec, -1\n"
        "v_exp_f32 v84, v84\n"
        "v_mfma_f32_32x32x16_bf16 v[64:79], v[116:119], v[128:131], v[48:63]\n"
        "v_exp_f32 v85, v85\n"
        "v_exp_f32 v86, v86\n"
        "v_exp_f32 v87, v87\n"
        "v_exp_f32 v88, v88\n"
        "v_exp_f32 v89, v89\n"
        "v_exp_f32 v90, v90\n"
        "v_smfmac_f32_32x32x32_bf16 v[16:31], v[120:123], v[80:87], v96\n"
        "buffer_load_dwordx4 v[164:167], v100, s[28:31], s74 offen offset:256 sc1\n"
        "s_add_u32 m0, s54, 115712\n"
        "s_add_u32 s57, s90, 69632\n"
        "buffer_load_dwordx4 v101, s[20:23], s57 offen lds\n"
        "v_exp_f32 v91, v91\n"
        "v_exp_f32 v92, v92\n"
        "v_exp_f32 v93, v93\n"
        "v_exp_f32 v94, v94\n"
        "v_exp_f32 v95, v95\n"
        "v_exp_f32 v64, v64\n"
        "v_exp_f32 v65, v65\n"
        "v_exp_f32 v66, v66\n"
        "v_smfmac_f32_32x32x32_bf16 v[16:31], v[140:143], v[88:95], v96\n"
        "v_exp_f32 v67, v67\n"
        "v_exp_f32 v68, v68\n"
        "s_waitcnt lgkmcnt(0)\n"
        "v_exp_f32 v69, v69\n"
        "v_mfma_f32_32x32x16_bf16 v[80:95], v[104:107], v[132:135], v[32:47]\n"
        "v_exp_f32 v70, v70\n"
        "v_exp_f32 v71, v71\n"
        "v_exp_f32 v72, v72\n"
        "v_exp_f32 v73, v73\n"
        "v_smfmac_f32_32x32x32_bf16 v[0:15], v[120:123], v[64:71], v96\n"
        "buffer_load_dwordx4 v[168:171], v100, s[28:31], s74 offen offset:2048 sc1\n"
        "v_exp_f32 v74, v74\n"
        "v_exp_f32 v75, v75\n"
        "v_exp_f32 v76, v76\n"
        "v_exp_f32 v77, v77\n"
        "v_exp_f32 v78, v78\n"
        "v_exp_f32 v79, v79\n"
        "s_nop 0\n"
        "v_exp_f32 v80, v80\n"
        "v_exp_f32 v81, v81\n"
        "v_exp_f32 v82, v82\n"
        "v_smfmac_f32_32x32x32_bf16 v[0:15], v[140:143], v[72:79], v96\n"
        "ds_read_b128 v[116:119], v97 offset:6144\n"
        "v_exp_f32 v83, v83\n"
        "s_mov_b64 exec, s[0:1]\n"
        "ds_read_b128 v[120:123], v98 offset:6144\n"
        "ds_read_b128 v[140:143], v98 offset:6656\n"
        "s_mov_b64 exec, -1\n"
        "v_exp_f32 v84, v84\n"
        "v_mfma_f32_32x32x16_bf16 v[64:79], v[104:107], v[128:131], v[48:63]\n"
        "v_exp_f32 v85, v85\n"
        "v_exp_f32 v86, v86\n"
        "v_exp_f32 v87, v87\n"
        "v_exp_f32 v88, v88\n"
        "v_exp_f32 v89, v89\n"
        "v_exp_f32 v90, v90\n"
        "v_smfmac_f32_32x32x32_bf16 v[16:31], v[108:111], v[80:87], v96\n"
        "buffer_load_dwordx4 v[172:175], v100, s[28:31], s74 offen offset:2304 sc1\n"
        "s_add_u32 m0, s54, 131072\n"
        "s_add_u32 s57, s90, 98304\n"
        "buffer_load_dwordx4 v101, s[20:23], s57 offen lds\n"
        "v_exp_f32 v91, v91\n"
        "v_exp_f32 v92, v92\n"
        "v_exp_f32 v93, v93\n"
        "v_exp_f32 v94, v94\n"
        "v_exp_f32 v95, v95\n"
        "v_exp_f32 v64, v64\n"
        "v_exp_f32 v65, v65\n"
        "v_exp_f32 v66, v66\n"
        "v_smfmac_f32_32x32x32_bf16 v[16:31], v[112:115], v[88:95], v96\n"
        "v_exp_f32 v67, v67\n"
        "v_exp_f32 v68, v68\n"
        "s_waitcnt lgkmcnt(0)\n"
        "v_exp_f32 v69, v69\n"
        "v_mfma_f32_32x32x16_bf16 v[80:95], v[116:119], v[132:135], v[32:47]\n"
        "v_exp_f32 v70, v70\n"
        "v_exp_f32 v71, v71\n"
        "v_exp_f32 v72, v72\n"
        "v_exp_f32 v73, v73\n"
        "v_smfmac_f32_32x32x32_bf16 v[0:15], v[108:111], v[64:71], v96\n"
        "s_add_u32 s74, s74, 0x1000\n"
        "buffer_load_dwordx4 v[176:179], v100, s[28:31], s74 offen sc1\n"
        "v_exp_f32 v74, v74\n"
        "v_exp_f32 v75, v75\n"
        "v_exp_f32 v76, v76\n"
        "v_exp_f32 v77, v77\n"
        "v_exp_f32 v78, v78\n"
        "v_exp_f32 v79, v79\n"
        "s_nop 0\n"
        "v_exp_f32 v80, v80\n"
        "v_exp_f32 v81, v81\n"
        "v_exp_f32 v82, v82\n"
        "v_smfmac_f32_32x32x32_bf16 v[0:15], v[112:115], v[72:79], v96\n"
        "ds_read_b128 v[104:107], v97 offset:16384\n"
        "v_exp_f32 v83, v83\n"
        "s_mov_b64 exec, s[0:1]\n"
        "ds_read_b128 v[108:111], v98 offset:16384\n"
        "ds_read_b128 v[112:115], v98 offset:16896\n"
        "s_mov_b64 exec, -1\n"
        "v_exp_f32 v84, v84\n"
        "v_mfma_f32_32x32x16_bf16 v[64:79], v[116:119], v[128:131], v[48:63]\n"
        "v_exp_f32 v85, v85\n"
        "v_exp_f32 v86, v86\n"
        "v_exp_f32 v87, v87\n"
        "v_exp_f32 v88, v88\n"
        "v_exp_f32 v89, v89\n"
        "v_exp_f32 v90, v90\n"
        "v_smfmac_f32_32x32x32_bf16 v[16:31], v[120:123], v[80:87], v96\n"
        "buffer_load_dwordx4 v[180:183], v100, s[28:31], s74 offen offset:256 sc1\n"
        "s_add_u32 m0, s54, 132096\n"
        "s_add_u32 s57, s90, 102400\n"
        "buffer_load_dwordx4 v101, s[20:23], s57 offen lds\n"
        "v_exp_f32 v91, v91\n"
        "v_exp_f32 v92, v92\n"
        "v_exp_f32 v93, v93\n"
        "v_exp_f32 v94, v94\n"
        "v_exp_f32 v95, v95\n"
        "v_exp_f32 v64, v64\n"
        "v_exp_f32 v65, v65\n"
        "v_exp_f32 v66, v66\n"
        "v_smfmac_f32_32x32x32_bf16 v[16:31], v[140:143], v[88:95], v96\n"
        "s_setprio 2\n"
        "v_exp_f32 v67, v67\n"
        "v_exp_f32 v68, v68\n"
        "s_waitcnt lgkmcnt(0)\n"
        "v_mfma_f32_32x32x16_bf16 v[80:95], v[104:107], v[132:135], v[32:47]\n"
        "v_exp_f32 v69, v69\n"
        "v_exp_f32 v70, v70\n"
        "v_exp_f32 v71, v71\n"
        "v_exp_f32 v72, v72\n"
        "v_exp_f32 v73, v73\n"
        "v_exp_f32 v74, v74\n"
        "v_smfmac_f32_32x32x32_bf16 v[0:15], v[120:123], v[64:71], v96\n"
        "buffer_load_dwordx4 v[184:187], v100, s[28:31], s74 offen offset:2048 sc1\n"
        "v_exp_f32 v75, v75\n"
        "v_exp_f32 v76, v76\n"
        "v_exp_f32 v77, v77\n"
        "v_exp_f32 v78, v78\n"
        "v_exp_f32 v79, v79\n"
        "v_exp_f32 v80, v80\n"
        "v_exp_f32 v81, v81\n"
        "v_exp_f32 v82, v82\n"
        "v_smfmac_f32_32x32x32_bf16 v[0:15], v[140:143], v[72:79], v96\n"
        "ds_read_b128 v[116:119], v97 offset:18432\n"
        "v_exp_f32 v83, v83\n"
        "s_mov_b64 exec, s[0:1]\n"
        "ds_read_b128 v[120:123], v98 offset:18432\n"
        "ds_read_b128 v[140:143], v98 offset:18944\n"
        "s_mov_b64 exec, -1\n"
        "v_exp_f32 v84, v84\n"
        "v_mfma_f32_32x32x16_bf16 v[64:79], v[104:107], v[128:131], v[48:63]\n"
        "v_exp_f32 v85, v85\n"
        "v_exp_f32 v86, v86\n"
        "v_exp_f32 v87, v87\n"
        "v_exp_f32 v88, v88\n"
        "v_exp_f32 v89, v89\n"
        "v_exp_f32 v90, v90\n"
        "v_smfmac_f32_32x32x32_bf16 v[16:31], v[108:111], v[80:87], v96\n"
        "buffer_load_dwordx4 v[188:191], v100, s[28:31], s74 offen offset:2304 sc1\n"
        "s_add_u32 m0, s54, 147456\n"
        "s_add_u32 s57, s90, 131072\n"
        "buffer_load_dwordx4 v101, s[20:23], s57 offen lds\n"
        "v_exp_f32 v91, v91\n"
        "v_exp_f32 v92, v92\n"
        "v_exp_f32 v93, v93\n"
        "v_exp_f32 v94, v94\n"
        "v_exp_f32 v95, v95\n"
        "v_exp_f32 v64, v64\n"
        "v_exp_f32 v65, v65\n"
        "v_exp_f32 v66, v66\n"
        "v_smfmac_f32_32x32x32_bf16 v[16:31], v[112:115], v[88:95], v96\n"
        "v_exp_f32 v67, v67\n"
        "v_exp_f32 v68, v68\n"
        "s_waitcnt lgkmcnt(0)\n"
        "v_exp_f32 v69, v69\n"
        "v_mfma_f32_32x32x16_bf16 v[80:95], v[116:119], v[132:135], v[32:47]\n"
        "v_exp_f32 v70, v70\n"
        "v_exp_f32 v71, v71\n"
        "v_exp_f32 v72, v72\n"
        "v_exp_f32 v73, v73\n"
        "v_smfmac_f32_32x32x32_bf16 v[0:15], v[108:111], v[64:71], v96\n"
        "s_add_u32 s74, s74, 0x1000\n"
        "buffer_load_dwordx4 v[192:195], v100, s[28:31], s74 offen sc1\n"
        "v_exp_f32 v74, v74\n"
        "v_exp_f32 v75, v75\n"
        "v_exp_f32 v76, v76\n"
        "v_exp_f32 v77, v77\n"
        "v_exp_f32 v78, v78\n"
        "v_exp_f32 v79, v79\n"
        "s_nop 0\n"
        "v_exp_f32 v80, v80\n"
        "v_exp_f32 v81, v81\n"
        "v_exp_f32 v82, v82\n"
        "v_smfmac_f32_32x32x32_bf16 v[0:15], v[112:115], v[72:79], v96\n"
        "ds_read_b128 v[104:107], v97 offset:20480\n"
        "v_exp_f32 v83, v83\n"
        "s_mov_b64 exec, s[0:1]\n"
        "ds_read_b128 v[108:111], v98 offset:20480\n"
        "ds_read_b128 v[112:115], v98 offset:20992\n"
        "s_mov_b64 exec, -1\n"
        "v_exp_f32 v84, v84\n"
        "v_mfma_f32_32x32x16_bf16 v[64:79], v[116:119], v[128:131], v[48:63]\n"
        "v_exp_f32 v85, v85\n"
        "v_exp_f32 v86, v86\n"
        "v_exp_f32 v87, v87\n"
        "v_exp_f32 v88, v88\n"
        "v_exp_f32 v89, v89\n"
        "v_exp_f32 v90, v90\n"
        "v_smfmac_f32_32x32x32_bf16 v[16:31], v[120:123], v[80:87], v96\n"
        "buffer_load_dwordx4 v[196:199], v100, s[28:31], s74 offen offset:256 sc1\n"
        "s_add_u32 m0, s54, 148480\n"
        "s_add_u32 s57, s90, 135168\n"
        "buffer_load_dwordx4 v101, s[20:23], s57 offen lds\n"
        "v_exp_f32 v91, v91\n"
        "v_exp_f32 v92, v92\n"
        "v_exp_f32 v93, v93\n"
        "v_exp_f32 v94, v94\n"
        "v_exp_f32 v95, v95\n"
        "v_exp_f32 v64, v64\n"
        "v_exp_f32 v65, v65\n"
        "v_exp_f32 v66, v66\n"
        "v_smfmac_f32_32x32x32_bf16 v[16:31], v[140:143], v[88:95], v96\n"
        "s_setprio 1\n"
        "v_exp_f32 v67, v67\n"
        "v_exp_f32 v68, v68\n"
        "s_waitcnt lgkmcnt(0)\n"
        "v_mfma_f32_32x32x16_bf16 v[80:95], v[104:107], v[132:135], v[32:47]\n"
        "v_exp_f32 v69, v69\n"
        "v_exp_f32 v70, v70\n"
        "v_exp_f32 v71, v71\n"
        "v_exp_f32 v72, v72\n"
        "v_exp_f32 v73, v73\n"
        "v_exp_f32 v74, v74\n"
        "v_smfmac_f32_32x32x32_bf16 v[0:15], v[120:123], v[64:71], v96\n"
        "buffer_load_dwordx4 v[200:203], v100, s[28:31], s74 offen offset:2048 sc1\n"
        "v_exp_f32 v75, v75\n"
        "v_exp_f32 v76, v76\n"
        "v_exp_f32 v77, v77\n"
        "v_exp_f32 v78, v78\n"
        "v_exp_f32 v79, v79\n"
        "v_exp_f32 v80, v80\n"
        "v_exp_f32 v81, v81\n"
        "v_exp_f32 v82, v82\n"
        "v_smfmac_f32_32x32x32_bf16 v[0:15], v[140:143], v[72:79], v96\n"
        "ds_read_b128 v[116:119], v97 offset:22528\n"
        "v_exp_f32 v83, v83\n"
        "s_mov_b64 exec, s[0:1]\n"
        "ds_read_b128 v[120:123], v98 offset:22528\n"
        "ds_read_b128 v[140:143], v98 offset:23040\n"
        "s_mov_b64 exec, -1\n"
        "v_exp_f32 v84, v84\n"
        "v_mfma_f32_32x32x16_bf16 v[64:79], v[104:107], v[128:131], v[48:63]\n"
        "v_exp_f32 v85, v85\n"
        "v_exp_f32 v86, v86\n"
        "v_exp_f32 v87, v87\n"
        "v_exp_f32 v88, v88\n"
        "v_exp_f32 v89, v89\n"
        "v_exp_f32 v90, v90\n"
        "v_smfmac_f32_32x32x32_bf16 v[16:31], v[108:111], v[80:87], v96\n"
        "buffer_load_dwordx4 v[204:207], v100, s[28:31], s74 offen offset:2304 sc1\n"
        "v_exp_f32 v91, v91\n"
        "v_exp_f32 v92, v92\n"
        "v_exp_f32 v93, v93\n"
        "v_exp_f32 v94, v94\n"
        "v_exp_f32 v95, v95\n"
        "v_exp_f32 v64, v64\n"
        "v_exp_f32 v65, v65\n"
        "v_exp_f32 v66, v66\n"
        "v_smfmac_f32_32x32x32_bf16 v[16:31], v[112:115], v[88:95], v96\n"
        "s_setprio 0\n"
        "v_exp_f32 v67, v67\n"
        "v_exp_f32 v68, v68\n"
        "s_waitcnt lgkmcnt(0)\n"
        "v_mfma_f32_32x32x16_bf16 v[80:95], v[116:119], v[132:135], v[32:47]\n"
        "v_exp_f32 v69, v69\n"
        "v_exp_f32 v70, v70\n"
        "v_exp_f32 v71, v71\n"
        "v_exp_f32 v72, v72\n"
        "v_exp_f32 v73, v73\n"
        "v_exp_f32 v74, v74\n"
        "v_smfmac_f32_32x32x32_bf16 v[0:15], v[108:111], v[64:71], v96\n"
        "s_add_u32 s74, s74, 0x1000\n"
        "buffer_load_dwordx4 v[208:211], v100, s[28:31], s74 offen sc1\n"
        "v_exp_f32 v75, v75\n"
        "v_exp_f32 v76, v76\n"
        "v_exp_f32 v77, v77\n"
        "v_exp_f32 v78, v78\n"
        "v_exp_f32 v79, v79\n"
        "v_exp_f32 v80, v80\n"
        "v_exp_f32 v81, v81\n"
        "v_exp_f32 v82, v82\n"
        "v_smfmac_f32_32x32x32_bf16 v[0:15], v[112:115], v[72:79], v96\n"
        "s_waitcnt vmcnt(17) lgkmcnt(0)\n"
        "s_barrier\n"
        "s_setprio 3\n"
        "v_exp_f32 v83, v83\n"
        "ds_read_b128 v[104:107], v97 offset:32768\n"
        "v_exp_f32 v84, v84\n"
        "s_mov_b64 exec, s[0:1]\n"
        "ds_read_b128 v[108:111], v98 offset:32768\n"
        "ds_read_b128 v[112:115], v98 offset:33280\n"
        "s_mov_b64 exec, -1\n"
        "v_exp_f32 v85, v85\n"
        "v_mfma_f32_32x32x16_bf16 v[64:79], v[116:119], v[128:131], v[48:63]\n"
        "v_exp_f32 v86, v86\n"
        "v_exp_f32 v87, v87\n"
        "v_exp_f32 v88, v88\n"
        "v_exp_f32 v89, v89\n"
        "v_smfmac_f32_32x32x32_bf16 v[16:31], v[120:123], v[80:87], v96\n"
        "s_mov_b32 m0, s54\n"
        "s_add_u32 s57, s90, 163840\n"
        "buffer_load_dwordx4 v101, s[20:23], s57 offen lds\n"
        "v_exp_f32 v90, v90\n"
        "v_exp_f32 v91, v91\n"
        "v_exp_f32 v92, v92\n"
        "v_exp_f32 v93, v93\n"
        "v_exp_f32 v94, v94\n"
        "v_exp_f32 v95, v95\n"
        "s_nop 0\n"
        "v_exp_f32 v64, v64\n"
        "v_exp_f32 v65, v65\n"
        "v_smfmac_f32_32x32x32_bf16 v[16:31], v[140:143], v[88:95], v96\n"
        "v_exp_f32 v66, v66\n"
        "v_exp_f32 v67, v67\n"
        "v_exp_f32 v68, v68\n"
        "s_waitcnt lgkmcnt(0)\n"
        "v_mfma_f32_32x32x16_bf16 v[80:95], v[104:107], v[132:135], v[32:47]\n"
        "v_exp_f32 v69, v69\n"
        "v_exp_f32 v70, v70\n"
        "v_exp_f32 v71, v71\n"
        "v_exp_f32 v72, v72\n"
        "v_exp_f32 v73, v73\n"
        "v_exp_f32 v74, v74\n"
        "v_smfmac_f32_32x32x32_bf16 v[0:15], v[120:123], v[64:71], v96\n"
        "s_add_u32 m0, s54, 1024\n"
        "s_add_u32 s57, s90, 167936\n"
        "buffer_load_dwordx4 v101, s[20:23], s57 offen lds\n"
        "v_exp_f32 v75, v75\n"
        "v_exp_f32 v76, v76\n"
        "v_exp_f32 v77, v77\n"
        "v_exp_f32 v78, v78\n"
        "v_exp_f32 v79, v79\n"
        "v_exp_f32 v80, v80\n"
        "v_exp_f32 v81, v81\n"
        "v_exp_f32 v82, v82\n"
        "v_smfmac_f32_32x32x32_bf16 v[0:15], v[140:143], v[72:79], v96\n"
        "ds_read_b128 v[116:119], v97 offset:34816\n"
        "v_exp_f32 v83, v83\n"
        "s_mov_b64 exec, s[0:1]\n"
        "ds_read_b128 v[120:123], v98 offset:34816\n"
        "ds_read_b128 v[140:143], v98 offset:35328\n"
        "s_mov_b64 exec, -1\n"
        "v_exp_f32 v84, v84\n"
        "v_mfma_f32_32x32x16_bf16 v[64:79], v[104:107], v[128:131], v[48:63]\n"
        "v_exp_f32 v85, v85\n"
        "v_exp_f32 v86, v86\n"
        "v_exp_f32 v87, v87\n"
        "v_exp_f32 v88, v88\n"
        "v_exp_f32 v89, v89\n"
        "v_exp_f32 v90, v90\n"
        "v_smfmac_f32_32x32x32_bf16 v[16:31], v[108:111], v[80:87], v96\n"
        "buffer_load_dwordx4 v[212:215], v100, s[28:31], s74 offen offset:256 sc1\n"
        "v_exp_f32 v91, v91\n"
        "v_exp_f32 v92, v92\n"
        "v_exp_f32 v93, v93\n"
        "v_exp_f32 v94, v94\n"
        "v_exp_f32 v95, v95\n"
        "v_exp_f32 v64, v64\n"
        "v_exp_f32 v65, v65\n"
        "v_exp_f32 v66, v66\n"
        "v_smfmac_f32_32x32x32_bf16 v[16:31], v[112:115], v[88:95], v96\n"
        "v_exp_f32 v67, v67\n"
        "v_exp_f32 v68, v68\n"
        "s_waitcnt lgkmcnt(0)\n"
        "v_exp_f32 v69, v69\n"
        "v_mfma_f32_32x32x16_bf16 v[80:95], v[116:119], v[132:135], v[32:47]\n"
        "v_exp_f32 v70, v70\n"
        "v_exp_f32 v71, v71\n"
        "v_exp_f32 v72, v72\n"
        "v_exp_f32 v73, v73\n"
        "v_smfmac_f32_32x32x32_bf16 v[0:15], v[108:111], v[64:71], v96\n"
        "buffer_load_dwordx4 v[216:219], v100, s[28:31], s74 offen offset:2048 sc1\n"
        "v_exp_f32 v74, v74\n"
        "v_exp_f32 v75, v75\n"
        "v_exp_f32 v76, v76\n"
        "v_exp_f32 v77, v77\n"
        "v_exp_f32 v78, v78\n"
        "v_exp_f32 v79, v79\n"
        "s_nop 0\n"
        "v_exp_f32 v80, v80\n"
        "v_exp_f32 v81, v81\n"
        "v_exp_f32 v82, v82\n"
        "v_smfmac_f32_32x32x32_bf16 v[0:15], v[112:115], v[72:79], v96\n"
        "s_setprio 2\n"
        "v_exp_f32 v83, v83\n"
        "ds_read_b128 v[104:107], v97 offset:36864\n"
        "v_exp_f32 v84, v84\n"
        "s_mov_b64 exec, s[0:1]\n"
        "ds_read_b128 v[108:111], v98 offset:36864\n"
        "ds_read_b128 v[112:115], v98 offset:37376\n"
        "s_mov_b64 exec, -1\n"
        "v_exp_f32 v85, v85\n"
        "v_mfma_f32_32x32x16_bf16 v[64:79], v[116:119], v[128:131], v[48:63]\n"
        "v_exp_f32 v86, v86\n"
        "v_exp_f32 v87, v87\n"
        "v_exp_f32 v88, v88\n"
        "v_exp_f32 v89, v89\n"
        "v_smfmac_f32_32x32x32_bf16 v[16:31], v[120:123], v[80:87], v96\n"
        "buffer_load_dwordx4 v[220:223], v100, s[28:31], s74 offen offset:2304 sc1\n"
        "v_exp_f32 v90, v90\n"
        "v_exp_f32 v91, v91\n"
        "v_exp_f32 v92, v92\n"
        "v_exp_f32 v93, v93\n"
        "v_exp_f32 v94, v94\n"
        "v_exp_f32 v95, v95\n"
        "s_nop 0\n"
        "v_exp_f32 v64, v64\n"
        "v_exp_f32 v65, v65\n"
        "v_exp_f32 v66, v66\n"
        "v_smfmac_f32_32x32x32_bf16 v[16:31], v[140:143], v[88:95], v96\n"
        "v_exp_f32 v67, v67\n"
        "v_exp_f32 v68, v68\n"
        "s_waitcnt lgkmcnt(0)\n"
        "v_exp_f32 v69, v69\n"
        "v_mfma_f32_32x32x16_bf16 v[80:95], v[104:107], v[132:135], v[32:47]\n"
        "v_exp_f32 v70, v70\n"
        "v_exp_f32 v71, v71\n"
        "v_exp_f32 v72, v72\n"
        "v_exp_f32 v73, v73\n"
        "v_smfmac_f32_32x32x32_bf16 v[0:15], v[120:123], v[64:71], v96\n"
        "s_add_u32 s74, s74, 0x1000\n"
        "buffer_load_dwordx4 v[224:227], v100, s[28:31], s74 offen sc1\n"
        "v_exp_f32 v74, v74\n"
        "v_exp_f32 v75, v75\n"
        "v_exp_f32 v76, v76\n"
        "v_exp_f32 v77, v77\n"
        "v_exp_f32 v78, v78\n"
        "v_exp_f32 v79, v79\n"
        "s_nop 0\n"
        "v_exp_f32 v80, v80\n"
        "v_exp_f32 v81, v81\n"
        "v_exp_f32 v82, v82\n"
        "v_smfmac_f32_32x32x32_bf16 v[0:15], v[140:143], v[72:79], v96\n"
        "s_setprio 1\n"
        "v_exp_f32 v83, v83\n"
        "ds_read_b128 v[116:119], v97 offset:38912\n"
        "v_exp_f32 v84, v84\n"
        "s_mov_b64 exec, s[0:1]\n"
        "ds_read_b128 v[120:123], v98 offset:38912\n"
        "ds_read_b128 v[140:143], v98 offset:39424\n"
        "s_mov_b64 exec, -1\n"
        "v_exp_f32 v85, v85\n"
        "v_mfma_f32_32x32x16_bf16 v[64:79], v[104:107], v[128:131], v[48:63]\n"
        "v_exp_f32 v86, v86\n"
        "v_exp_f32 v87, v87\n"
        "v_exp_f32 v88, v88\n"
        "v_exp_f32 v89, v89\n"
        "v_smfmac_f32_32x32x32_bf16 v[16:31], v[108:111], v[80:87], v96\n"
        "buffer_load_dwordx4 v[228:231], v100, s[28:31], s74 offen offset:256 sc1\n"
        "v_exp_f32 v90, v90\n"
        "v_exp_f32 v91, v91\n"
        "v_exp_f32 v92, v92\n"
        "v_exp_f32 v93, v93\n"
        "v_exp_f32 v94, v94\n"
        "v_exp_f32 v95, v95\n"
        "s_nop 0\n"
        "v_exp_f32 v64, v64\n"
        "v_exp_f32 v65, v65\n"
        "v_exp_f32 v66, v66\n"
        "v_smfmac_f32_32x32x32_bf16 v[16:31], v[112:115], v[88:95], v96\n"
        "s_setprio 0\n"
        "v_exp_f32 v67, v67\n"
        "v_exp_f32 v68, v68\n"
        "s_waitcnt lgkmcnt(0)\n"
        "v_mfma_f32_32x32x16_bf16 v[80:95], v[116:119], v[132:135], v[32:47]\n"
        "v_exp_f32 v69, v69\n"
        "v_exp_f32 v70, v70\n"
        "v_exp_f32 v71, v71\n"
        "v_exp_f32 v72, v72\n"
        "v_exp_f32 v73, v73\n"
        "v_exp_f32 v74, v74\n"
        "v_smfmac_f32_32x32x32_bf16 v[0:15], v[108:111], v[64:71], v96\n"
        "buffer_load_dwordx4 v[232:235], v100, s[28:31], s74 offen offset:2048 sc1\n"
        "v_exp_f32 v75, v75\n"
        "v_exp_f32 v76, v76\n"
        "v_exp_f32 v77, v77\n"
        "v_exp_f32 v78, v78\n"
        "v_exp_f32 v79, v79\n"
        "v_exp_f32 v80, v80\n"
        "v_exp_f32 v81, v81\n"
        "v_exp_f32 v82, v82\n"
        "v_smfmac_f32_32x32x32_bf16 v[0:15], v[112:115], v[72:79], v96\n"
        "s_waitcnt vmcnt(25) lgkmcnt(0)\n"
        "s_barrier\n"
        "s_setprio 3\n"
        "v_exp_f32 v83, v83\n"
        "ds_read_b128 v[104:107], v97 offset:49152\n"
        "v_exp_f32 v84, v84\n"
        "s_mov_b64 exec, s[0:1]\n"
        "ds_read_b128 v[108:111], v98 offset:49152\n"
        "ds_read_b128 v[112:115], v98 offset:49664\n"
        "s_mov_b64 exec, -1\n"
        "v_exp_f32 v85, v85\n"
        "v_mfma_f32_32x32x16_bf16 v[64:79], v[116:119], v[128:131], v[48:63]\n"
        "v_exp_f32 v86, v86\n"
        "v_exp_f32 v87, v87\n"
        "v_exp_f32 v88, v88\n"
        "v_exp_f32 v89, v89\n"
        "v_smfmac_f32_32x32x32_bf16 v[16:31], v[120:123], v[80:87], v96\n"
        "s_add_u32 m0, s54, 32768\n"
        "s_add_u32 s57, s90, 196608\n"
        "buffer_load_dwordx4 v101, s[20:23], s57 offen lds\n"
        "v_exp_f32 v90, v90\n"
        "v_exp_f32 v91, v91\n"
        "v_exp_f32 v92, v92\n"
        "v_exp_f32 v93, v93\n"
        "v_exp_f32 v94, v94\n"
        "v_exp_f32 v95, v95\n"
        "s_nop 0\n"
        "v_exp_f32 v64, v64\n"
        "v_exp_f32 v65, v65\n"
        "v_exp_f32 v66, v66\n"
        "v_smfmac_f32_32x32x32_bf16 v[16:31], v[140:143], v[88:95], v96\n"
        "v_exp_f32 v67, v67\n"
        "v_exp_f32 v68, v68\n"
        "s_waitcnt lgkmcnt(0)\n"
        "v_exp_f32 v69, v69\n"
        "v_mfma_f32_32x32x16_bf16 v[80:95], v[104:107], v[132:135], v[32:47]\n"
        "v_exp_f32 v70, v70\n"
        "v_exp_f32 v71, v71\n"
        "v_exp_f32 v72, v72\n"
        "v_exp_f32 v73, v73\n"
        "v_smfmac_f32_32x32x32_bf16 v[0:15], v[120:123], v[64:71], v96\n"
        "s_add_u32 m0, s54, 33792\n"
        "s_add_u32 s57, s90, 200704\n"
        "buffer_load_dwordx4 v101, s[20:23], s57 offen lds\n"
        "v_exp_f32 v74, v74\n"
        "v_exp_f32 v75, v75\n"
        "v_exp_f32 v76, v76\n"
        "v_exp_f32 v77, v77\n"
        "v_exp_f32 v78, v78\n"
        "v_exp_f32 v79, v79\n"
        "s_nop 0\n"
        "v_exp_f32 v80, v80\n"
        "v_exp_f32 v81, v81\n"
        "v_exp_f32 v82, v82\n"
        "v_smfmac_f32_32x32x32_bf16 v[0:15], v[140:143], v[72:79], v96\n"
        "ds_read_b128 v[116:119], v97 offset:51200\n"
        "v_exp_f32 v83, v83\n"
        "s_mov_b64 exec, s[0:1]\n"
        "ds_read_b128 v[120:123], v98 offset:51200\n"
        "ds_read_b128 v[140:143], v98 offset:51712\n"
        "s_mov_b64 exec, -1\n"
        "v_exp_f32 v84, v84\n"
        "v_mfma_f32_32x32x16_bf16 v[64:79], v[104:107], v[128:131], v[48:63]\n"
        "v_exp_f32 v85, v85\n"
        "v_exp_f32 v86, v86\n"
        "v_exp_f32 v87, v87\n"
        "v_exp_f32 v88, v88\n"
        "v_exp_f32 v89, v89\n"
        "v_exp_f32 v90, v90\n"
        "v_smfmac_f32_32x32x32_bf16 v[16:31], v[108:111], v[80:87], v96\n"
        "buffer_load_dwordx4 v[236:239], v100, s[28:31], s74 offen offset:2304 sc1\n"
        "v_add_u32 v125, s54, v124\n"
        "s_lshl_b32 s57, s52, 8\n"
        "s_add_u32 s57, s57, 73728\n"
        "v_lshrrev_b32 v136, 2, v124\n"
        "v_add_u32 v136, s57, v136\n"
        "v_exp_f32 v91, v91\n"
        "v_exp_f32 v92, v92\n"
        "v_exp_f32 v93, v93\n"
        "v_exp_f32 v94, v94\n"
        "v_exp_f32 v95, v95\n"
        "v_exp_f32 v64, v64\n"
        "v_exp_f32 v65, v65\n"
        "v_exp_f32 v66, v66\n"
        "v_smfmac_f32_32x32x32_bf16 v[16:31], v[112:115], v[88:95], v96\n"
        "v_exp_f32 v67, v67\n"
        "v_exp_f32 v68, v68\n"
        "s_waitcnt lgkmcnt(0)\n"
        "v_exp_f32 v69, v69\n"
        "v_mfma_f32_32x32x16_bf16 v[80:95], v[116:119], v[132:135], v[32:47]\n"
        "v_exp_f32 v70, v70\n"
        "v_exp_f32 v71, v71\n"
        "v_exp_f32 v72, v72\n"
        "v_exp_f32 v73, v73\n"
        "v_smfmac_f32_32x32x32_bf16 v[0:15], v[108:111], v[64:71], v96\n"
        "s_add_u32 s74, s74, 0x1000\n"
        "buffer_load_dwordx4 v[240:243], v100, s[28:31], s74 offen sc1\n"
        "v_exp_f32 v74, v74\n"
        "v_exp_f32 v75, v75\n"
        "v_exp_f32 v76, v76\n"
        "v_exp_f32 v77, v77\n"
        "v_exp_f32 v78, v78\n"
        "v_exp_f32 v79, v79\n"
        "s_nop 0\n"
        "v_exp_f32 v80, v80\n"
        "v_exp_f32 v81, v81\n"
        "v_exp_f32 v82, v82\n"
        "v_smfmac_f32_32x32x32_bf16 v[0:15], v[112:115], v[72:79], v96\n"
        "ds_read_b128 v[104:107], v97 offset:53248\n"
        "v_exp_f32 v83, v83\n"
        "s_mov_b64 exec, s[0:1]\n"
        "ds_read_b128 v[108:111], v98 offset:53248\n"
        "ds_read_b128 v[112:115], v98 offset:53760\n"
        "s_mov_b64 exec, -1\n"
        "v_exp_f32 v84, v84\n"
        "v_mfma_f32_32x32x16_bf16 v[64:79], v[116:119], v[128:131], v[48:63]\n"
        "v_exp_f32 v85, v85\n"
        "v_exp_f32 v86, v86\n"
        "v_exp_f32 v87, v87\n"
        "v_exp_f32 v88, v88\n"
        "v_exp_f32 v89, v89\n"
        "v_exp_f32 v90, v90\n"
        "v_smfmac_f32_32x32x32_bf16 v[16:31], v[120:123], v[80:87], v96\n"
        "buffer_load_dwordx4 v[244:247], v100, s[28:31], s74 offen offset:256 sc1\n"
        "v_exp_f32 v91, v91\n"
        "v_exp_f32 v92, v92\n"
        "v_exp_f32 v93, v93\n"
        "v_exp_f32 v94, v94\n"
        "v_exp_f32 v95, v95\n"
        "v_exp_f32 v64, v64\n"
        "v_exp_f32 v65, v65\n"
        "v_exp_f32 v66, v66\n"
        "v_smfmac_f32_32x32x32_bf16 v[16:31], v[140:143], v[88:95], v96\n"
        "s_setprio 2\n"
        "v_exp_f32 v67, v67\n"
        "v_exp_f32 v68, v68\n"
        "s_waitcnt lgkmcnt(0)\n"
        "v_mfma_f32_32x32x16_bf16 v[80:95], v[104:107], v[132:135], v[32:47]\n"
        "ds_read_b128 v[116:119], v97 offset:55296\n"
        "v_exp_f32 v69, v69\n"
        "v_exp_f32 v70, v70\n"
        "v_exp_f32 v71, v71\n"
        "v_exp_f32 v72, v72\n"
        "v_exp_f32 v73, v73\n"
        "v_exp_f32 v74, v74\n"
        "v_smfmac_f32_32x32x32_bf16 v[0:15], v[120:123], v[64:71], v96\n"
        "buffer_load_dwordx4 v[248:251], v100, s[28:31], s74 offen offset:2048 sc1\n"
        "v_exp_f32 v75, v75\n"
        "v_exp_f32 v76, v76\n"
        "v_exp_f32 v77, v77\n"
        "v_exp_f32 v78, v78\n"
        "v_exp_f32 v79, v79\n"
        "v_exp_f32 v80, v80\n"
        "v_exp_f32 v81, v81\n"
        "v_exp_f32 v82, v82\n"
        "v_smfmac_f32_32x32x32_bf16 v[0:15], v[140:143], v[72:79], v96\n"
        "v_exp_f32 v83, v83\n"
        "s_mov_b64 exec, s[0:1]\n"
        "ds_read_b128 v[120:123], v98 offset:55296\n"
        "ds_read_b128 v[140:143], v98 offset:55808\n"
        "s_mov_b64 exec, -1\n"
        "v_exp_f32 v84, v84\n"
        "s_waitcnt lgkmcnt(2)\n"
        "v_exp_f32 v85, v85\n"
        "v_mfma_f32_32x32x16_bf16 v[64:79], v[116:119], v[132:135], v[32:47]\n"
        "v_exp_f32 v86, v86\n"
        "v_exp_f32 v87, v87\n"
        "v_exp_f32 v88, v88\n"
        "v_exp_f32 v89, v89\n"
        "v_smfmac_f32_32x32x32_bf16 v[16:31], v[108:111], v[80:87], v96\n"
        "buffer_load_dwordx4 v[252:255], v100, s[28:31], s74 offen offset:2304 sc1\n"
        "v_exp_f32 v90, v90\n"
        "v_exp_f32 v91, v91\n"
        "v_exp_f32 v92, v92\n"
        "v_exp_f32 v93, v93\n"
        "v_exp_f32 v94, v94\n"
        "v_exp_f32 v95, v95\n"
        "s_nop 0\n"
        "v_exp_f32 v64, v64\n"
        "v_exp_f32 v65, v65\n"
        "v_exp_f32 v66, v66\n"
        "v_smfmac_f32_32x32x32_bf16 v[16:31], v[112:115], v[88:95], v96\n"
        "s_setprio 1\n"
        "v_exp_f32 v67, v67\n"
        "v_exp_f32 v68, v68\n"
        "s_waitcnt lgkmcnt(0)\n"
        "v_mfma_f32_32x32x16_bf16 v[80:95], v[104:107], v[128:131], v[48:63]\n"
        "v_exp_f32 v69, v69\n"
        "v_exp_f32 v70, v70\n"
        "v_exp_f32 v71, v71\n"
        "v_exp_f32 v72, v72\n"
        "v_exp_f32 v73, v73\n"
        "v_exp_f32 v74, v74\n"
        "v_smfmac_f32_32x32x32_bf16 v[16:31], v[120:123], v[64:71], v96\n"
        "v_exp_f32 v75, v75\n"
        "v_exp_f32 v76, v76\n"
        "v_exp_f32 v77, v77\n"
        "v_exp_f32 v78, v78\n"
        "v_exp_f32 v79, v79\n"
        "v_exp_f32 v80, v80\n"
        "v_exp_f32 v81, v81\n"
        "v_exp_f32 v82, v82\n"
        "v_smfmac_f32_32x32x32_bf16 v[16:31], v[140:143], v[72:79], v96\n"
        "s_setprio 0\n"
        "v_exp_f32 v83, v83\n"
        "v_exp_f32 v84, v84\n"
        "v_exp_f32 v85, v85\n"
        "v_mfma_f32_32x32x16_bf16 v[64:79], v[116:119], v[128:131], v[48:63]\n"
        "v_exp_f32 v86, v86\n"
        "v_exp_f32 v87, v87\n"
        "v_exp_f32 v88, v88\n"
        "v_exp_f32 v89, v89\n"
        "v_smfmac_f32_32x32x32_bf16 v[0:15], v[108:111], v[80:87], v96\n"
        "v_exp_f32 v90, v90\n"
        "v_exp_f32 v91, v91\n"
        "ds_write_b128 v125, v[16:19] offset:16384\n"
        "ds_write_b128 v125, v[20:23] offset:17408\n"
        "ds_write_b32 v136, v24\n"
        "v_exp_f32 v92, v92\n"
        "v_exp_f32 v93, v93\n"
        "v_exp_f32 v94, v94\n"
        "v_exp_f32 v95, v95\n"
        "s_nop 0\n"
        "v_exp_f32 v64, v64\n"
        "v_exp_f32 v65, v65\n"
        "v_exp_f32 v66, v66\n"
        "v_smfmac_f32_32x32x32_bf16 v[0:15], v[112:115], v[88:95], v96\n"
        "s_waitcnt vmcnt(5) lgkmcnt(0)\n"
        "s_barrier\n"
        "s_setprio 1\n"
        "v_xor_b32 v125, 0x2000, v125\n"
        "v_xor_b32 v136, 0x400, v136\n"
        "ds_read_b32 v24, v136\n"
        "ds_read_b128 v[16:19], v125 offset:16384\n"
        "ds_read_b128 v[20:23], v125 offset:17408\n"
        "v_xor_b32 v97, 32, v99\n"
        "v_xor_b32 v98, 64, v99\n"
        "v_xor_b32 v102, 96, v99\n"
        "ds_read_b64_tr_b16 v[104:105], v99 offset:16384\n"
        "ds_read_b64_tr_b16 v[106:107], v99 offset:17408\n"
        "ds_read_b64_tr_b16 v[108:109], v97 offset:16384\n"
        "ds_read_b64_tr_b16 v[110:111], v97 offset:17408\n"
        "ds_read_b64_tr_b16 v[112:113], v98 offset:16384\n"
        "ds_read_b64_tr_b16 v[114:115], v98 offset:17408\n"
        "ds_read_b64_tr_b16 v[116:117], v102 offset:16384\n"
        "ds_read_b64_tr_b16 v[118:119], v102 offset:17408\n"
        "v_exp_f32 v67, v67\n"
        "v_exp_f32 v68, v68\n"
        "v_exp_f32 v69, v69\n"
        "v_exp_f32 v70, v70\n"
        "v_exp_f32 v71, v71\n"
        "v_exp_f32 v72, v72\n"
        "v_exp_f32 v73, v73\n"
        "v_smfmac_f32_32x32x32_bf16 v[0:15], v[120:123], v[64:71], v96\n"
        "s_add_u32 m0, s54, 49152\n"
        "s_add_u32 s57, s90, 229376\n"
        "buffer_load_dwordx4 v101, s[20:23], s57 offen lds\n"
        "s_add_u32 m0, s54, 50176\n"
        "s_add_u32 s57, s90, 233472\n"
        "buffer_load_dwordx4 v101, s[20:23], s57 offen lds\n"
        "v_exp_f32 v74, v74\n"
        "v_exp_f32 v75, v75\n"
        "v_exp_f32 v76, v76\n"
        "v_exp_f32 v77, v77\n"
        "v_exp_f32 v78, v78\n"
        "v_exp_f32 v79, v79\n"
        "v_lshrrev_b32 v134, 4, v124\n"
        "s_nop 0\n"
        "v_smfmac_f32_32x32x32_bf16 v[0:15], v[140:143], v[72:79], v96\n"
        "s_waitcnt lgkmcnt(6)\n"
        "v_mfma_f32_16x16x32_bf16 v[32:35], v[104:107], v[144:147], 0\n"
        "ds_read_b64_tr_b16 v[120:121], v99 offset:32768\n"
        "v_mfma_f32_16x16x32_bf16 v[36:39], v[104:107], v[148:151], 0\n"
        "ds_read_b64_tr_b16 v[122:123], v99 offset:33792\n"
        "s_nop 6\n"
        "v_add_f32 v8, v8, v24\n"
        "v_add_f32 v0, v0, v16\n"
        "v_add_f32 v1, v1, v17\n"
        "v_add_f32 v2, v2, v18\n"
        "v_add_f32 v3, v3, v19\n"
        "v_add_f32 v4, v4, v20\n"
        "v_add_f32 v5, v5, v21\n"
        "v_add_f32 v6, v6, v22\n"
        "v_add_f32 v7, v7, v23\n"
        "s_waitcnt lgkmcnt(6)\n"
        "v_mfma_f32_16x16x32_bf16 v[40:43], v[108:111], v[144:147], 0\n"
        "ds_read_b64_tr_b16 v[104:105], v97 offset:32768\n"
        "v_rcp_f32 v9, v8\n"
        "v_mul_f32 v10, 0, v0\n"
        "v_mul_f32 v11, 0, v1\n"
        "v_fmac_f32 v10, 0, v2\n"
        "v_fmac_f32 v11, 0, v3\n"
        "v_fmac_f32 v10, 0, v4\n"
        "v_fmac_f32 v11, 0, v5\n"
        "v_fmac_f32 v10, 0, v6\n"
        "v_fmac_f32 v11, 0, v7\n"
        "v_mfma_f32_16x16x32_bf16 v[44:47], v[108:111], v[148:151], 0\n"
        "ds_read_b64_tr_b16 v[106:107], v97 offset:33792\n"
        "v_cmp_ge_f32 vcc, 0x5f800000, v8\n"
        "v_cmp_eq_f32_e64 s[40:41], v10, v11\n"
        "s_nop 0\n"
        "s_and_b64 vcc, vcc, s[40:41]\n"
        "s_cmp_eq_u64 vcc, exec\n"
        "s_cbranch_scc0 LSLOW%=\n"
        "LBACK%=:\n"
        "v_mul_f32 v0, v9, v0\n"
        "v_mul_f32 v1, v9, v1\n"
        "v_mul_f32 v2, v9, v2\n"
        "v_mul_f32 v3, v9, v3\n"
        "v_mul_f32 v4, v9, v4\n"
        "v_mul_f32 v5, v9, v5\n"
        "v_mul_f32 v6, v9, v6\n"
        "v_mul_f32 v7, v9, v7\n"
        "v_cvt_pk_bf16_f32 v16, v0, v1\n"
        "s_waitcnt lgkmcnt(6)\n"
        "v_mfma_f32_16x16x32_bf16 v[48:51], v[112:115], v[144:147], 0\n"
        "ds_read_b64_tr_b16 v[108:109], v98 offset:32768\n"
        "v_cvt_pk_bf16_f32 v17, v2, v3\n"
        "v_cvt_pk_bf16_f32 v18, v4, v5\n"
        "v_cvt_pk_bf16_f32 v19, v6, v7\n"
        "buffer_store_dwordx4 v[16:19], v124, s[28:31], s73 offen sc1\n"
        "s_setprio 0\n"
        "v_mfma_f32_16x16x32_bf16 v[52:55], v[112:115], v[148:151], 0\n"
        "ds_read_b64_tr_b16 v[110:111], v98 offset:33792\n"
        "s_waitcnt lgkmcnt(6)\n"
        "v_mfma_f32_16x16x32_bf16 v[56:59], v[116:119], v[144:147], 0\n"
        "ds_read_b64_tr_b16 v[112:113], v102 offset:32768\n"
        "v_mfma_f32_16x16x32_bf16 v[60:63], v[116:119], v[148:151], 0\n"
        "ds_read_b64_tr_b16 v[114:115], v102 offset:33792\n"
        "s_waitcnt lgkmcnt(4)\n"
        "v_mfma_f32_16x16x32_bf16 v[32:35], v[120:123], v[152:155], v[32:35]\n"
        "s_waitcnt vmcnt(8) lgkmcnt(0)\n"
        "s_barrier\n"
        "s_setprio 3\n"
        "ds_read_b64_tr_b16 v[116:117], v99 offset:49152\n"
        "v_mfma_f32_16x16x32_bf16 v[36:39], v[120:123], v[156:159], v[36:39]\n"
        "ds_read_b64_tr_b16 v[118:119], v99 offset:50176\n"
        "s_add_u32 m0, s54, 16384\n"
        "s_add_u32 s57, s90, 262144\n"
        "buffer_load_dwordx4 v101, s[20:23], s57 offen lds\n"
        "v_add_u32 v24, 0x10000, v99\n"
        "v_mfma_f32_16x16x32_bf16 v[40:43], v[104:107], v[152:155], v[40:43]\n"
        "ds_read_b64_tr_b16 v[120:121], v97 offset:49152\n"
        "v_add_u32 v25, 0x10000, v97\n"
        "v_mfma_f32_16x16x32_bf16 v[44:47], v[104:107], v[156:159], v[44:47]\n"
        "ds_read_b64_tr_b16 v[122:123], v97 offset:50176\n"
        "v_add_u32 v26, 0x10000, v98\n"
        "v_mfma_f32_16x16x32_bf16 v[48:51], v[108:111], v[152:155], v[48:51]\n"
        "ds_read_b64_tr_b16 v[104:105], v98 offset:49152\n"
        "v_add_u32 v27, 0x10000, v102\n"
        "v_mfma_f32_16x16x32_bf16 v[52:55], v[108:111], v[156:159], v[52:55]\n"
        "ds_read_b64_tr_b16 v[106:107], v98 offset:50176\n"
        "s_add_u32 m0, s54, 17408\n"
        "s_add_u32 s57, s90, 266240\n"
        "buffer_load_dwordx4 v101, s[20:23], s57 offen lds\n"
        "v_subrev_u32 v20, 0x10000, v99\n"
        "v_mfma_f32_16x16x32_bf16 v[56:59], v[112:115], v[152:155], v[56:59]\n"
        "ds_read_b64_tr_b16 v[108:109], v102 offset:49152\n"
        "v_subrev_u32 v21, 0x10000, v97\n"
        "v_mfma_f32_16x16x32_bf16 v[60:63], v[112:115], v[156:159], v[60:63]\n"
        "ds_read_b64_tr_b16 v[110:111], v102 offset:50176\n"
        "v_subrev_u32 v22, 0x10000, v98\n"
        "s_waitcnt lgkmcnt(4)\n"
        "v_mfma_f32_16x16x32_bf16 v[32:35], v[116:119], v[160:163], v[32:35]\n"
        "ds_read_b64_tr_b16 v[112:113], v24 offset:0\n"
        "v_subrev_u32 v23, 0x10000, v102\n"
        "v_mfma_f32_16x16x32_bf16 v[36:39], v[116:119], v[164:167], v[36:39]\n"
        "ds_read_b64_tr_b16 v[114:115], v24 offset:1024\n"
        "s_add_u32 m0, s54, 65536\n"
        "s_add_u32 s57, s90, 294912\n"
        "buffer_load_dwordx4 v101, s[20:23], s57 offen lds\n"
        "s_lshr_b32 s59, s86, 2\n"
        "s_lshl_b32 s59, s59, 2\n"
        "v_mfma_f32_16x16x32_bf16 v[40:43], v[120:123], v[160:163], v[40:43]\n"
        "ds_read_b64_tr_b16 v[116:117], v25 offset:0\n"
        "s_add_u32 s59, s59, s89\n"
        "s_lshl_b32 s59, s59, 10\n"
        "v_mfma_f32_16x16x32_bf16 v[44:47], v[120:123], v[164:167], v[44:47]\n"
        "ds_read_b64_tr_b16 v[118:119], v25 offset:1024\n"
        "s_add_u32 s59, s59, 16973824\n"
        "v_lshrrev_b32 v135, 3, v134\n"
        "s_waitcnt lgkmcnt(4)\n"
        "v_mfma_f32_16x16x32_bf16 v[48:51], v[104:107], v[160:163], v[48:51]\n"
        "s_setprio 2\n"
        "ds_read_b64_tr_b16 v[120:121], v26 offset:0\n"
        "v_lshl_add_u32 v132, v135, 7, s59\n"
        "v_mfma_f32_16x16x32_bf16 v[52:55], v[104:107], v[164:167], v[52:55]\n"
        "ds_read_b64_tr_b16 v[122:123], v26 offset:1024\n"
        "s_add_u32 m0, s54, 66560\n"
        "s_add_u32 s57, s90, 299008\n"
        "buffer_load_dwordx4 v101, s[20:23], s57 offen lds\n"
        "v_and_b32 v135, 7, v134\n"
        "v_mfma_f32_16x16x32_bf16 v[56:59], v[108:111], v[160:163], v[56:59]\n"
        "ds_read_b64_tr_b16 v[104:105], v27 offset:0\n"
        "s_add_u32 s60, s75, 0x1000\n"
        "v_and_b32 v0, 15, v134\n"
        "v_mfma_f32_16x16x32_bf16 v[60:63], v[108:111], v[164:167], v[60:63]\n"
        "ds_read_b64_tr_b16 v[106:107], v27 offset:1024\n"
        "s_lshl_b32 s59, s89, 1\n"
        "s_add_u32 s59, s59, s88\n"
        "s_waitcnt lgkmcnt(4)\n"
        "v_mfma_f32_16x16x32_bf16 v[32:35], v[112:115], v[168:171], v[32:35]\n"
        "ds_read_b64_tr_b16 v[108:109], v24 offset:16384\n"
        "s_lshl_b32 s59, s59, 13\n"
        "s_add_u32 s59, s59, 0x10000\n"
        "v_mfma_f32_16x16x32_bf16 v[36:39], v[112:115], v[172:175], v[36:39]\n"
        "ds_read_b64_tr_b16 v[110:111], v24 offset:17408\n"
        "s_add_u32 m0, s54, 81920\n"
        "s_add_u32 s57, s90, 327680\n"
        "buffer_load_dwordx4 v101, s[20:23], s57 offen lds\n"
        "v_lshlrev_b32 v0, 8, v0\n"
        "v_mfma_f32_16x16x32_bf16 v[40:43], v[116:119], v[168:171], v[40:43]\n"
        "s_setprio 1\n"
        "ds_read_b64_tr_b16 v[112:113], v25 offset:16384\n"
        "v_lshrrev_b32 v1, 4, v134\n"
        "v_mfma_f32_16x16x32_bf16 v[44:47], v[116:119], v[172:175], v[44:47]\n"
        "ds_read_b64_tr_b16 v[114:115], v25 offset:17408\n"
        "v_xor_b32 v1, v1, v135\n"
        "s_waitcnt lgkmcnt(4)\n"
        "v_mfma_f32_16x16x32_bf16 v[48:51], v[120:123], v[168:171], v[48:51]\n"
        "ds_read_b64_tr_b16 v[116:117], v26 offset:16384\n"
        "v_lshlrev_b32 v1, 4, v1\n"
        "v_mfma_f32_16x16x32_bf16 v[52:55], v[120:123], v[172:175], v[52:55]\n"
        "ds_read_b64_tr_b16 v[118:119], v26 offset:17408\n"
        "s_add_u32 m0, s54, 82944\n"
        "s_add_u32 s57, s90, 331776\n"
        "buffer_load_dwordx4 v101, s[20:23], s57 offen lds\n"
        "v_add3_u32 v0, v0, v1, s59\n"
        "v_mfma_f32_16x16x32_bf16 v[56:59], v[104:107], v[168:171], v[56:59]\n"
        "s_setprio 0\n"
        "ds_read_b64_tr_b16 v[120:121], v27 offset:16384\n"
        "v_xor_b32 v6, 64, v0\n"
        "v_mfma_f32_16x16x32_bf16 v[60:63], v[104:107], v[172:175], v[60:63]\n"
        "ds_read_b64_tr_b16 v[122:123], v27 offset:17408\n"
        "v_lshrrev_b32 v4, 3, v134\n"
        "s_waitcnt lgkmcnt(4)\n"
        "v_mfma_f32_16x16x32_bf16 v[32:35], v[108:111], v[176:179], v[32:35]\n"
        "s_waitcnt vmcnt(4) lgkmcnt(0)\n"
        "s_barrier\n"
        "s_cmp_lg_u32 s52, 0\n"
        "s_cbranch_scc1 LO%=\n"
        "s_mov_b64 exec, 1\n"
        "v_mov_b32 v138, s48\n"
        "v_mov_b32 v139, s49\n"
        "buffer_store_dwordx2 v[138:139], off, s[28:31], s68 sc1\n"
        "s_mov_b64 exec, -1\n"
        "LO%=:\n"
        "s_setprio 3\n"
        "ds_read_b64_tr_b16 v[104:105], v20 offset:0\n"
        "v_lshlrev_b32 v2, 1, v135\n"
        "v_mfma_f32_16x16x32_bf16 v[36:39], v[108:111], v[180:183], v[36:39]\n"
        "ds_read_b64_tr_b16 v[106:107], v20 offset:1024\n"
        "s_add_u32 m0, s54, 98304\n"
        "s_add_u32 s57, s90, 360448\n"
        "buffer_load_dwordx4 v101, s[20:23], s57 offen lds\n"
        "v_xor_b32 v2, v2, v4\n"
        "v_mfma_f32_16x16x32_bf16 v[40:43], v[112:115], v[176:179], v[40:43]\n"
        "ds_read_b64_tr_b16 v[108:109], v21 offset:0\n"
        "v_lshlrev_b32 v2, 4, v2\n"
        "v_mfma_f32_16x16x32_bf16 v[44:47], v[112:115], v[180:183], v[44:47]\n"
        "ds_read_b64_tr_b16 v[110:111], v21 offset:1024\n"
        "v_lshl_add_u32 v2, v4, 8, v2\n"
        "v_mfma_f32_16x16x32_bf16 v[48:51], v[116:119], v[176:179], v[48:51]\n"
        "ds_read_b64_tr_b16 v[112:113], v22 offset:0\n"
        "s_add_u32 s59, s54, 0x10000\n"
        "v_add_u32 v2, s59, v2\n"
        "v_mfma_f32_16x16x32_bf16 v[52:55], v[116:119], v[180:183], v[52:55]\n"
        "ds_read_b64_tr_b16 v[114:115], v22 offset:1024\n"
        "s_add_u32 m0, s54, 99328\n"
        "s_add_u32 s57, s90, 364544\n"
        "buffer_load_dwordx4 v101, s[20:23], s57 offen lds\n"
        "v_xor_b32 v3, 16, v2\n"
        "v_mfma_f32_16x16x32_bf16 v[56:59], v[120:123], v[176:179], v[56:59]\n"
        "ds_read_b64_tr_b16 v[116:117], v23 offset:0\n"
        "v_lshlrev_b32 v4, 10, v4\n"
        "v_mfma_f32_16x16x32_bf16 v[60:63], v[120:123], v[180:183], v[60:63]\n"
        "ds_read_b64_tr_b16 v[118:119], v23 offset:1024\n"
        "v_lshl_add_u32 v4, v135, 4, v4\n"
        "s_waitcnt lgkmcnt(4)\n"
        "v_mfma_f32_16x16x32_bf16 v[32:35], v[104:107], v[184:187], v[32:35]\n"
        "ds_read_b64_tr_b16 v[120:121], v20 offset:32768\n"
        "s_lshl_b32 s59, s86, 3\n"
        "s_add_u32 s59, s59, s52\n"
        "v_mfma_f32_16x16x32_bf16 v[36:39], v[104:107], v[188:191], v[36:39]\n"
        "ds_read_b64_tr_b16 v[122:123], v20 offset:33792\n"
        "s_add_u32 m0, s54, 114688\n"
        "s_add_u32 s57, s90, 393216\n"
        "buffer_load_dwordx4 v101, s[20:23], s57 offen lds\n"
        "s_lshl_b32 s59, s59, 13\n"
        "s_lshl_b32 s58, s87, 7\n"
        "v_mfma_f32_16x16x32_bf16 v[40:43], v[108:111], v[184:187], v[40:43]\n"
        "ds_read_b64_tr_b16 v[104:105], v21 offset:32768\n"
        "s_add_u32 s71, s59, s58\n"
        "v_mfma_f32_16x16x32_bf16 v[44:47], v[108:111], v[188:191], v[44:47]\n"
        "ds_read_b64_tr_b16 v[106:107], v21 offset:33792\n"
        "s_waitcnt lgkmcnt(4)\n"
        "v_mfma_f32_16x16x32_bf16 v[48:51], v[112:115], v[184:187], v[48:51]\n"
        "ds_read_b64_tr_b16 v[108:109], v22 offset:32768\n"
        "v_mfma_f32_16x16x32_bf16 v[52:55], v[112:115], v[188:191], v[52:55]\n"
        "ds_read_b64_tr_b16 v[110:111], v22 offset:33792\n"
        "s_add_u32 m0, s54, 115712\n"
        "s_add_u32 s57, s90, 397312\n"
        "buffer_load_dwordx4 v101, s[20:23], s57 offen lds\n"
        "v_mfma_f32_16x16x32_bf16 v[56:59], v[116:119], v[184:187], v[56:59]\n"
        "ds_read_b64_tr_b16 v[112:113], v23 offset:32768\n"
        "v_mfma_f32_16x16x32_bf16 v[60:63], v[116:119], v[188:191], v[60:63]\n"
        "ds_read_b64_tr_b16 v[114:115], v23 offset:33792\n"
        "s_waitcnt lgkmcnt(4)\n"
        "v_mfma_f32_16x16x32_bf16 v[32:35], v[120:123], v[192:195], v[32:35]\n"
        "s_setprio 2\n"
        "ds_read_b64_tr_b16 v[116:117], v20 offset:49152\n"
        "v_mfma_f32_16x16x32_bf16 v[36:39], v[120:123], v[196:199], v[36:39]\n"
        "ds_read_b64_tr_b16 v[118:119], v20 offset:50176\n"
        "s_add_u32 m0, s54, 131072\n"
        "s_add_u32 s57, s90, 425984\n"
        "buffer_load_dwordx4 v101, s[20:23], s57 offen lds\n"
        "v_mfma_f32_16x16x32_bf16 v[40:43], v[104:107], v[192:195], v[40:43]\n"
        "ds_read_b64_tr_b16 v[120:121], v21 offset:49152\n"
        "v_mfma_f32_16x16x32_bf16 v[44:47], v[104:107], v[196:199], v[44:47]\n"
        "ds_read_b64_tr_b16 v[122:123], v21 offset:50176\n"
        "s_waitcnt lgkmcnt(4)\n"
        "v_mfma_f32_16x16x32_bf16 v[48:51], v[108:111], v[192:195], v[48:51]\n"
        "ds_read_b64_tr_b16 v[104:105], v22 offset:49152\n"
        "v_mfma_f32_16x16x32_bf16 v[52:55], v[108:111], v[196:199], v[52:55]\n"
        "ds_read_b64_tr_b16 v[106:107], v22 offset:50176\n"
        "s_add_u32 m0, s54, 132096\n"
        "s_add_u32 s57, s90, 430080\n"
        "buffer_load_dwordx4 v101, s[20:23], s57 offen lds\n"
        "v_mfma_f32_16x16x32_bf16 v[56:59], v[112:115], v[192:195], v[56:59]\n"
        "ds_read_b64_tr_b16 v[108:109], v23 offset:49152\n"
        "v_mfma_f32_16x16x32_bf16 v[60:63], v[112:115], v[196:199], v[60:63]\n"
        "ds_read_b64_tr_b16 v[110:111], v23 offset:50176\n"
        "s_waitcnt lgkmcnt(4)\n"
        "v_mfma_f32_16x16x32_bf16 v[32:35], v[116:119], v[200:203], v[32:35]\n"
        "s_setprio 1\n"
        "ds_read_b64_tr_b16 v[112:113], v20 offset:16384\n"
        "v_mfma_f32_16x16x32_bf16 v[36:39], v[116:119], v[204:207], v[36:39]\n"
        "ds_read_b64_tr_b16 v[114:115], v20 offset:17408\n"
        "s_add_u32 m0, s54, 147456\n"
        "s_add_u32 s57, s91, 0\n"
        "buffer_load_dwordx4 v101, s[24:27], s57 offen lds\n"
        "v_mfma_f32_16x16x32_bf16 v[40:43], v[120:123], v[200:203], v[40:43]\n"
        "ds_read_b64_tr_b16 v[116:117], v21 offset:16384\n"
        "v_mfma_f32_16x16x32_bf16 v[44:47], v[120:123], v[204:207], v[44:47]\n"
        "ds_read_b64_tr_b16 v[118:119], v21 offset:17408\n"
        "s_waitcnt lgkmcnt(4)\n"
        "v_mfma_f32_16x16x32_bf16 v[48:51], v[104:107], v[200:203], v[48:51]\n"
        "ds_read_b64_tr_b16 v[120:121], v22 offset:16384\n"
        "v_mfma_f32_16x16x32_bf16 v[52:55], v[104:107], v[204:207], v[52:55]\n"
        "ds_read_b64_tr_b16 v[122:123], v22 offset:17408\n"
        "s_add_u32 m0, s54, 148480\n"
        "s_add_u32 s57, s91, 4096\n"
        "buffer_load_dwordx4 v101, s[24:27], s57 offen lds\n"
        "v_mfma_f32_16x16x32_bf16 v[56:59], v[108:111], v[200:203], v[56:59]\n"
        "s_setprio 0\n"
        "ds_read_b64_tr_b16 v[104:105], v23 offset:16384\n"
        "v_mfma_f32_16x16x32_bf16 v[60:63], v[108:111], v[204:207], v[60:63]\n"
        "ds_read_b64_tr_b16 v[106:107], v23 offset:17408\n"
        "buffer_load_dwordx2 v[138:139], v132, s[28:31], 0 offen sc1\n"
        "s_waitcnt lgkmcnt(4)\n"
        "v_mfma_f32_16x16x32_bf16 v[32:35], v[112:115], v[208:211], v[32:35]\n"
        "s_waitcnt vmcnt(5) lgkmcnt(0)\n"
        "s_barrier\n"
        "s_setprio 3\n"
        "ds_read_b64_tr_b16 v[108:109], v99 offset:0\n"
        "v_mfma_f32_16x16x32_bf16 v[36:39], v[112:115], v[212:215], v[36:39]\n"
        "ds_read_b64_tr_b16 v[110:111], v99 offset:1024\n"
        "s_mov_b32 m0, s54\n"
        "s_add_u32 s57, s91, 32768\n"
        "buffer_load_dwordx4 v101, s[24:27], s57 offen lds\n"
        "v_mfma_f32_16x16x32_bf16 v[40:43], v[116:119], v[208:211], v[40:43]\n"
        "ds_read_b64_tr_b16 v[112:113], v97 offset:0\n"
        "v_mfma_f32_16x16x32_bf16 v[44:47], v[116:119], v[212:215], v[44:47]\n"
        "ds_read_b64_tr_b16 v[114:115], v97 offset:1024\n"
        "v_mfma_f32_16x16x32_bf16 v[48:51], v[120:123], v[208:211], v[48:51]\n"
        "ds_read_b64_tr_b16 v[116:117], v98 offset:0\n"
        "v_mfma_f32_16x16x32_bf16 v[52:55], v[120:123], v[212:215], v[52:55]\n"
        "ds_read_b64_tr_b16 v[118:119], v98 offset:1024\n"
        "s_add_u32 m0, s54, 1024\n"
        "s_add_u32 s57, s91, 36864\n"
        "buffer_load_dwordx4 v101, s[24:27], s57 offen lds\n"
        "v_mfma_f32_16x16x32_bf16 v[56:59], v[104:107], v[208:211], v[56:59]\n"
        "ds_read_b64_tr_b16 v[120:121], v102 offset:0\n"
        "v_mfma_f32_16x16x32_bf16 v[60:63], v[104:107], v[212:215], v[60:63]\n"
        "ds_read_b64_tr_b16 v[122:123], v102 offset:1024\n"
        "s_add_u32 m0, s54, 32768\n"
        "s_add_u32 s57, s91, 65536\n"
        "buffer_load_dwordx4 v101, s[24:27], s57 offen lds\n"
        "s_waitcnt lgkmcnt(4)\n"
        "v_mfma_f32_16x16x32_bf16 v[32:35], v[108:111], v[216:219], v[32:35]\n"
        "ds_read_b64_tr_b16 v[104:105], v99 offset:16384\n"
        "v_mfma_f32_16x16x32_bf16 v[36:39], v[108:111], v[220:223], v[36:39]\n"
        "ds_read_b64_tr_b16 v[106:107], v99 offset:17408\n"
        "v_mov_b32 v28, v137\n"
        "v_mfma_f32_16x16x32_bf16 v[40:43], v[112:115], v[216:219], v[40:43]\n"
        "ds_read_b64_tr_b16 v[108:109], v97 offset:16384\n"
        "v_mov_b32 v30, v137\n"
        "v_mfma_f32_16x16x32_bf16 v[44:47], v[112:115], v[220:223], v[44:47]\n"
        "ds_read_b64_tr_b16 v[110:111], v97 offset:17408\n"
        "s_add_u32 m0, s54, 33792\n"
        "s_add_u32 s57, s91, 69632\n"
        "buffer_load_dwordx4 v101, s[24:27], s57 offen lds\n"
        "s_nop 1\n"
        "v_permlane16_swap_b32 v28, v30\n"
        "s_waitcnt lgkmcnt(4)\n"
        "v_mfma_f32_16x16x32_bf16 v[48:51], v[116:119], v[216:219], v[48:51]\n"
        "ds_read_b64_tr_b16 v[112:113], v98 offset:16384\n"
        "s_nop 1\n"
        "v_sub_f32 v29, 1.0, v28\n"
        "v_mfma_f32_16x16x32_bf16 v[52:55], v[116:119], v[220:223], v[52:55]\n"
        "ds_read_b64_tr_b16 v[114:115], v98 offset:17408\n"
        "v_sub_f32 v31, 1.0, v30\n"
        "v_mfma_f32_16x16x32_bf16 v[56:59], v[120:123], v[216:219], v[56:59]\n"
        "ds_read_b64_tr_b16 v[116:117], v102 offset:16384\n"
        "v_mfma_f32_16x16x32_bf16 v[60:63], v[120:123], v[220:223], v[60:63]\n"
        "ds_read_b64_tr_b16 v[118:119], v102 offset:17408\n"
        "s_add_u32 m0, s54, 49152\n"
        "s_add_u32 s57, s91, 98304\n"
        "buffer_load_dwordx4 v101, s[24:27], s57 offen lds\n"
        "s_waitcnt lgkmcnt(4)\n"
        "v_mfma_f32_16x16x32_bf16 v[32:35], v[104:107], v[224:227], v[32:35]\n"
        "s_setprio 2\n"
        "ds_read_b64_tr_b16 v[120:121], v99 offset:32768\n"
        "v_mfma_f32_16x16x32_bf16 v[36:39], v[104:107], v[228:231], v[36:39]\n"
        "ds_read_b64_tr_b16 v[122:123], v99 offset:33792\n"
        "v_mfma_f32_16x16x32_bf16 v[40:43], v[108:111], v[224:227], v[40:43]\n"
        "ds_read_b64_tr_b16 v[104:105], v97 offset:32768\n"
        "v_mfma_f32_16x16x32_bf16 v[44:47], v[108:111], v[228:231], v[44:47]\n"
        "ds_read_b64_tr_b16 v[106:107], v97 offset:33792\n"
        "s_add_u32 m0, s54, 50176\n"
        "s_add_u32 s57, s91, 102400\n"
        "buffer_load_dwordx4 v101, s[24:27], s57 offen lds\n"
        "s_waitcnt lgkmcnt(4)\n"
        "v_mfma_f32_16x16x32_bf16 v[48:51], v[112:115], v[224:227], v[48:51]\n"
        "ds_read_b64_tr_b16 v[108:109], v98 offset:32768\n"
        "v_mfma_f32_16x16x32_bf16 v[52:55], v[112:115], v[228:231], v[52:55]\n"
        "ds_read_b64_tr_b16 v[110:111], v98 offset:33792\n"
        "s_waitcnt vmcnt(6)\n"
        "v_cmp_ne_u64_e64 vcc, v[138:139], s[48:49]\n"
        "s_mov_b32 s77, 0x100000\n"
        "s_cmp_eq_u64 vcc, 0\n"
        "s_cbranch_scc1 LJQ%=\n"
        "LJP%=:\n"
        "s_sleep 1\n"
        "buffer_load_dwordx2 v[138:139], v132, s[28:31], 0 offen sc1\n"
        "s_sub_u32 s77, s77, 1\n"
        "s_cmp_eq_u32 s77, 0\n"
        "s_waitcnt vmcnt(0)\n"
        "s_cbranch_scc1 LJQ%=\n"
        "v_cmp_ne_u64_e64 vcc, v[138:139], s[48:49]\n"
        "s_nop 0\n"
        "s_cmp_eq_u64 vcc, 0\n"
        "s_cbranch_scc0 LJP%=\n"
        "LJQ%=:\n"
        "buffer_load_dwordx4 v[144:147], v100, s[28:31], s75 offen sc1\n"
        "v_mfma_f32_16x16x32_bf16 v[56:59], v[116:119], v[224:227], v[56:59]\n"
        "ds_read_b64_tr_b16 v[112:113], v102 offset:32768\n"
        "v_mfma_f32_16x16x32_bf16 v[60:63], v[116:119], v[228:231], v[60:63]\n"
        "ds_read_b64_tr_b16 v[114:115], v102 offset:33792\n"
        "s_waitcnt lgkmcnt(4)\n"
        "v_mfma_f32_16x16x32_bf16 v[32:35], v[120:123], v[232:235], v[32:35]\n"
        "s_setprio 1\n"
        "ds_read_b64_tr_b16 v[116:117], v99 offset:49152\n"
        "v_mfma_f32_16x16x32_bf16 v[36:39], v[120:123], v[236:239], v[36:39]\n"
        "ds_read_b64_tr_b16 v[118:119], v99 offset:50176\n"
        "buffer_load_dwordx4 v[148:151], v100, s[28:31], s75 offen offset:256 sc1\n"
        "v_mfma_f32_16x16x32_bf16 v[40:43], v[104:107], v[232:235], v[40:43]\n"
        "ds_read_b64_tr_b16 v[120:121], v97 offset:49152\n"
        "v_mfma_f32_16x16x32_bf16 v[44:47], v[104:107], v[236:239], v[44:47]\n"
        "ds_read_b64_tr_b16 v[122:123], v97 offset:50176\n"
        "s_waitcnt lgkmcnt(4)\n"
        "v_mfma_f32_16x16x32_bf16 v[48:51], v[108:111], v[232:235], v[48:51]\n"
        "ds_read_b64_tr_b16 v[104:105], v98 offset:49152\n"
        "v_mfma_f32_16x16x32_bf16 v[52:55], v[108:111], v[236:239], v[52:55]\n"
        "ds_read_b64_tr_b16 v[106:107], v98 offset:50176\n"
        "buffer_load_dwordx4 v[152:155], v100, s[28:31], s75 offen offset:2048 sc1\n"
        "v_mfma_f32_16x16x32_bf16 v[56:59], v[112:115], v[232:235], v[56:59]\n"
        "s_setprio 0\n"
        "ds_read_b64_tr_b16 v[108:109], v102 offset:49152\n"
        "v_mfma_f32_16x16x32_bf16 v[60:63], v[112:115], v[236:239], v[60:63]\n"
        "ds_read_b64_tr_b16 v[110:111], v102 offset:50176\n"
        "s_waitcnt lgkmcnt(4)\n"
        "v_mfma_f32_16x16x32_bf16 v[32:35], v[116:119], v[240:243], v[32:35]\n"
        "s_waitcnt vmcnt(7) lgkmcnt(0)\n"
        "s_barrier\n"
        "s_setprio 3\n"
        "ds_read_b64_tr_b16 v[112:113], v24 offset:0\n"
        "v_mfma_f32_16x16x32_bf16 v[36:39], v[116:119], v[244:247], v[36:39]\n"
        "ds_read_b64_tr_b16 v[114:115], v24 offset:1024\n"
        "buffer_load_dwordx4 v[156:159], v100, s[28:31], s75 offen offset:2304 sc1\n"
        "v_mfma_f32_16x16x32_bf16 v[40:43], v[120:123], v[240:243], v[40:43]\n"
        "ds_read_b64_tr_b16 v[116:117], v25 offset:0\n"
        "v_mfma_f32_16x16x32_bf16 v[44:47], v[120:123], v[244:247], v[44:47]\n"
        "ds_read_b64_tr_b16 v[118:119], v25 offset:1024\n"
        "v_mfma_f32_16x16x32_bf16 v[48:51], v[104:107], v[240:243], v[48:51]\n"
        "ds_read_b64_tr_b16 v[120:121], v26 offset:0\n"
        "v_mfma_f32_16x16x32_bf16 v[52:55], v[104:107], v[244:247], v[52:55]\n"
        "ds_read_b64_tr_b16 v[122:123], v26 offset:1024\n"
        "buffer_load_dwordx4 v[160:163], v100, s[28:31], s60 offen sc1\n"
        "v_mfma_f32_16x16x32_bf16 v[56:59], v[108:111], v[240:243], v[56:59]\n"
        "ds_read_b64_tr_b16 v[104:105], v27 offset:0\n"
        "v_mfma_f32_16x16x32_bf16 v[60:63], v[108:111], v[244:247], v[60:63]\n"
        "ds_read_b64_tr_b16 v[106:107], v27 offset:1024\n"
        "s_waitcnt lgkmcnt(4)\n"
        "v_mfma_f32_16x16x32_bf16 v[32:35], v[112:115], v[248:251], v[32:35]\n"
        "ds_read_b64_tr_b16 v[108:109], v24 offset:16384\n"
        "v_mfma_f32_16x16x32_bf16 v[36:39], v[112:115], v[252:255], v[36:39]\n"
        "ds_read_b64_tr_b16 v[110:111], v24 offset:17408\n"
        "buffer_load_dwordx4 v[164:167], v100, s[28:31], s60 offen offset:256 sc1\n"
        "v_mfma_f32_16x16x32_bf16 v[40:43], v[116:119], v[248:251], v[40:43]\n"
        "ds_read_b64_tr_b16 v[112:113], v25 offset:16384\n"
        "v_mfma_f32_16x16x32_bf16 v[44:47], v[116:119], v[252:255], v[44:47]\n"
        "ds_read_b64_tr_b16 v[114:115], v25 offset:17408\n"
        "s_waitcnt lgkmcnt(4)\n"
        "v_mfma_f32_16x16x32_bf16 v[48:51], v[120:123], v[248:251], v[48:51]\n"
        "s_setprio 2\n"
        "ds_read_b64_tr_b16 v[116:117], v26 offset:16384\n"
        "v_mfma_f32_16x16x32_bf16 v[52:55], v[120:123], v[252:255], v[52:55]\n"
        "ds_read_b64_tr_b16 v[118:119], v26 offset:17408\n"
        "buffer_load_dwordx4 v[168:171], v100, s[28:31], s60 offen offset:2048 sc1\n"
        "v_mfma_f32_16x16x32_bf16 v[56:59], v[104:107], v[248:251], v[56:59]\n"
        "ds_read_b64_tr_b16 v[120:121], v27 offset:16384\n"
        "v_mfma_f32_16x16x32_bf16 v[60:63], v[104:107], v[252:255], v[60:63]\n"
        "ds_read_b64_tr_b16 v[122:123], v27 offset:17408\n"
        "s_waitcnt lgkmcnt(4)\n"
        "s_waitcnt vmcnt(6)\n"
        "v_mfma_f32_16x16x32_bf16 v[64:67], v[108:111], v[144:147], 0\n"
        "ds_read_b64_tr_b16 v[104:105], v20 offset:0\n"
        "s_waitcnt vmcnt(5)\n"
        "v_mfma_f32_16x16x32_bf16 v[68:71], v[108:111], v[148:151], 0\n"
        "ds_read_b64_tr_b16 v[106:107], v20 offset:1024\n"
        "buffer_load_dwordx4 v[172:175], v100, s[28:31], s60 offen offset:2304 sc1\n"
        "v_mul_f32 v32, v29, v32\n"
        "v_mul_f32 v33, v29, v33\n"
        "v_mfma_f32_16x16x32_bf16 v[72:75], v[112:115], v[144:147], 0\n"
        "s_setprio 1\n"
        "ds_read_b64_tr_b16 v[108:109], v21 offset:0\n"
        "v_mul_f32 v34, v29, v34\n"
        "v_mul_f32 v35, v29, v35\n"
        "v_mfma_f32_16x16x32_bf16 v[76:79], v[112:115], v[148:151], 0\n"
        "ds_read_b64_tr_b16 v[110:111], v21 offset:1024\n"
        "v_mul_f32 v36, v31, v36\n"
        "v_mul_f32 v37, v31, v37\n"
        "s_waitcnt lgkmcnt(4)\n"
        "v_mfma_f32_16x16x32_bf16 v[80:83], v[116:119], v[144:147], 0\n"
        "ds_read_b64_tr_b16 v[112:113], v22 offset:0\n"
        "v_mul_f32 v38, v31, v38\n"
        "v_mul_f32 v39, v31, v39\n"
        "v_mfma_f32_16x16x32_bf16 v[84:87], v[116:119], v[148:151], 0\n"
        "ds_read_b64_tr_b16 v[114:115], v22 offset:1024\n"
        "v_mul_f32 v40, v29, v40\n"
        "v_mul_f32 v41, v29, v41\n"
        "v_mfma_f32_16x16x32_bf16 v[88:91], v[120:123], v[144:147], 0\n"
        "s_setprio 0\n"
        "ds_read_b64_tr_b16 v[116:117], v23 offset:0\n"
        "v_mul_f32 v42, v29, v42\n"
        "v_mul_f32 v43, v29, v43\n"
        "v_mfma_f32_16x16x32_bf16 v[92:95], v[120:123], v[148:151], 0\n"
        "ds_read_b64_tr_b16 v[118:119], v23 offset:1024\n"
        "v_mul_f32 v44, v31, v44\n"
        "v_mul_f32 v45, v31, v45\n"
        "s_waitcnt lgkmcnt(4)\n"
        "s_waitcnt vmcnt(5)\n"
        "v_mfma_f32_16x16x32_bf16 v[64:67], v[104:107], v[152:155], v[64:67]\n"
        "s_waitcnt vmcnt(5) lgkmcnt(0)\n"
        "s_barrier\n"
        "s_or_b32 s57, s2, s52\n"
        "s_cmp_lg_u32 s57, 0\n"
        "s_cbranch_scc1 LEP%=\n"
        "s_mov_b64 exec, 1\n"
        "v_mov_b32 v138, s48\n"
        "v_mov_b32 v139, s49\n"
        "s_mov_b32 s57, 16777216\n"
        "buffer_store_dwordx2 v[138:139], off, s[28:31], s57 sc1\n"
        "s_mov_b64 exec, -1\n"
        "LEP%=:\n"
        "s_setprio 3\n"
        "ds_read_b64_tr_b16 v[120:121], v20 offset:32768\n"
        "v_mul_f32 v46, v31, v46\n"
        "v_mul_f32 v47, v31, v47\n"
        "s_waitcnt vmcnt(4)\n"
        "v_mfma_f32_16x16x32_bf16 v[68:71], v[104:107], v[156:159], v[68:71]\n"
        "ds_read_b64_tr_b16 v[122:123], v20 offset:33792\n"
        "v_mul_f32 v48, v29, v48\n"
        "v_mul_f32 v49, v29, v49\n"
        "v_mfma_f32_16x16x32_bf16 v[72:75], v[108:111], v[152:155], v[72:75]\n"
        "ds_read_b64_tr_b16 v[104:105], v21 offset:32768\n"
        "v_mul_f32 v50, v29, v50\n"
        "v_mul_f32 v51, v29, v51\n"
        "v_mfma_f32_16x16x32_bf16 v[76:79], v[108:111], v[156:159], v[76:79]\n"
        "ds_read_b64_tr_b16 v[106:107], v21 offset:33792\n"
        "v_mul_f32 v52, v31, v52\n"
        "v_mul_f32 v53, v31, v53\n"
        "v_mfma_f32_16x16x32_bf16 v[80:83], v[112:115], v[152:155], v[80:83]\n"
        "ds_read_b64_tr_b16 v[108:109], v20 offset:49152\n"
        "v_mul_f32 v54, v31, v54\n"
        "v_mul_f32 v55, v31, v55\n"
        "v_mfma_f32_16x16x32_bf16 v[84:87], v[112:115], v[156:159], v[84:87]\n"
        "ds_read_b64_tr_b16 v[110:111], v20 offset:50176\n"
        "v_mul_f32 v56, v29, v56\n"
        "v_mul_f32 v57, v29, v57\n"
        "v_mfma_f32_16x16x32_bf16 v[88:91], v[116:119], v[152:155], v[88:91]\n"
        "ds_read_b64_tr_b16 v[112:113], v21 offset:49152\n"
        "v_mul_f32 v58, v29, v58\n"
        "v_mul_f32 v59, v29, v59\n"
        "v_mfma_f32_16x16x32_bf16 v[92:95], v[116:119], v[156:159], v[92:95]\n"
        "ds_read_b64_tr_b16 v[114:115], v21 offset:50176\n"
        "v_mul_f32 v60, v31, v60\n"
        "v_mul_f32 v61, v31, v61\n"
        "s_waitcnt lgkmcnt(4)\n"
        "s_waitcnt vmcnt(3)\n"
        "v_mfma_f32_16x16x32_bf16 v[64:67], v[120:123], v[160:163], v[64:67]\n"
        "ds_read_b64_tr_b16 v[116:117], v22 offset:32768\n"
        "v_mul_f32 v62, v31, v62\n"
        "v_mul_f32 v63, v31, v63\n"
        "s_waitcnt vmcnt(2)\n"
        "v_mfma_f32_16x16x32_bf16 v[68:71], v[120:123], v[164:167], v[68:71]\n"
        "ds_read_b64_tr_b16 v[118:119], v22 offset:33792\n"
        "v_mfma_f32_16x16x32_bf16 v[72:75], v[104:107], v[160:163], v[72:75]\n"
        "ds_read_b64_tr_b16 v[120:121], v23 offset:32768\n"
        "v_mfma_f32_16x16x32_bf16 v[76:79], v[104:107], v[164:167], v[76:79]\n"
        "ds_read_b64_tr_b16 v[122:123], v23 offset:33792\n"
        "s_waitcnt lgkmcnt(4)\n"
        "s_waitcnt vmcnt(1)\n"
        "v_mfma_f32_16x16x32_bf16 v[64:67], v[108:111], v[168:171], v[64:67]\n"
        "s_setprio 2\n"
        "ds_read_b64_tr_b16 v[104:105], v22 offset:49152\n"
        "s_waitcnt vmcnt(0)\n"
        "v_mfma_f32_16x16x32_bf16 v[68:71], v[108:111], v[172:175], v[68:71]\n"
        "ds_read_b64_tr_b16 v[106:107], v22 offset:50176\n"
        "v_mfma_f32_16x16x32_bf16 v[72:75], v[112:115], v[168:171], v[72:75]\n"
        "ds_read_b64_tr_b16 v[108:109], v23 offset:49152\n"
        "v_mfma_f32_16x16x32_bf16 v[76:79], v[112:115], v[172:175], v[76:79]\n"
        "ds_read_b64_tr_b16 v[110:111], v23 offset:50176\n"
        "s_waitcnt lgkmcnt(4)\n"
        "v_mfma_f32_16x16x32_bf16 v[80:83], v[116:119], v[160:163], v[80:83]\n"
        "v_mfma_f32_16x16x32_bf16 v[84:87], v[116:119], v[164:167], v[84:87]\n"
        "s_nop 0\n"
        "v_pk_fma_f32 v[32:33], v[64:65], v[28:29], v[32:33] op_sel_hi:[1,0,1]\n"
        "v_pk_fma_f32 v[34:35], v[66:67], v[28:29], v[34:35] op_sel_hi:[1,0,1]\n"
        "v_mfma_f32_16x16x32_bf16 v[88:91], v[120:123], v[160:163], v[88:91]\n"
        "s_setprio 1\n"
        "ds_write_b128 v0, v[32:35] offset:0\n"
        "v_pk_fma_f32 v[36:37], v[68:69], v[30:31], v[36:37] op_sel_hi:[1,0,1]\n"
        "v_pk_fma_f32 v[38:39], v[70:71], v[30:31], v[38:39] op_sel_hi:[1,0,1]\n"
        "ds_write_b128 v0, v[36:39] offset:4096\n"
        "v_mfma_f32_16x16x32_bf16 v[92:95], v[120:123], v[164:167], v[92:95]\n"
        "v_pk_fma_f32 v[40:41], v[72:73], v[28:29], v[40:41] op_sel_hi:[1,0,1]\n"
        "v_pk_fma_f32 v[42:43], v[74:75], v[28:29], v[42:43] op_sel_hi:[1,0,1]\n"
        "ds_write_b128 v6, v[40:43] offset:0\n"
        "v_pk_fma_f32 v[44:45], v[76:77], v[30:31], v[44:45] op_sel_hi:[1,0,1]\n"
        "s_waitcnt lgkmcnt(3)\n"
        "v_mfma_f32_16x16x32_bf16 v[80:83], v[104:107], v[168:171], v[80:83]\n"
        "s_setprio 0\n"
        "v_pk_fma_f32 v[46:47], v[78:79], v[30:31], v[46:47] op_sel_hi:[1,0,1]\n"
        "ds_write_b128 v6, v[44:47] offset:4096\n"
        "v_mfma_f32_16x16x32_bf16 v[84:87], v[104:107], v[172:175], v[84:87]\n"
        "v_mfma_f32_16x16x32_bf16 v[88:91], v[108:111], v[168:171], v[88:91]\n"
        "v_mfma_f32_16x16x32_bf16 v[92:95], v[108:111], v[172:175], v[92:95]\n"
        "s_nop 5\n"
        "s_nop 0\n"
        "v_pk_fma_f32 v[48:49], v[80:81], v[28:29], v[48:49] op_sel_hi:[1,0,1]\n"
        "v_pk_fma_f32 v[50:51], v[82:83], v[28:29], v[50:51] op_sel_hi:[1,0,1]\n"
        "ds_write_b128 v0, v[48:51] offset:128\n"
        "v_pk_fma_f32 v[52:53], v[84:85], v[30:31], v[52:53] op_sel_hi:[1,0,1]\n"
        "v_pk_fma_f32 v[54:55], v[86:87], v[30:31], v[54:55] op_sel_hi:[1,0,1]\n"
        "ds_write_b128 v0, v[52:55] offset:4224\n"
        "v_pk_fma_f32 v[56:57], v[88:89], v[28:29], v[56:57] op_sel_hi:[1,0,1]\n"
        "v_pk_fma_f32 v[58:59], v[90:91], v[28:29], v[58:59] op_sel_hi:[1,0,1]\n"
        "ds_write_b128 v6, v[56:59] offset:128\n"
        "v_pk_fma_f32 v[60:61], v[92:93], v[30:31], v[60:61] op_sel_hi:[1,0,1]\n"
        "v_pk_fma_f32 v[62:63], v[94:95], v[30:31], v[62:63] op_sel_hi:[1,0,1]\n"
        "ds_write_b128 v6, v[60:63] offset:4224\n"
        "s_waitcnt lgkmcnt(0)\n"
        "s_barrier\n"
        "ds_read_b128 v[32:35], v2 offset:0\n"
        "ds_read_b128 v[36:39], v3 offset:0\n"
        "ds_read_b128 v[40:43], v2 offset:16384\n"
        "ds_read_b128 v[44:47], v3 offset:16384\n"
        "ds_read_b128 v[48:51], v2 offset:32768\n"
        "ds_read_b128 v[52:55], v3 offset:32768\n"
        "ds_read_b128 v[56:59], v2 offset:49152\n"
        "ds_read_b128 v[60:63], v3 offset:49152\n"
        "s_waitcnt lgkmcnt(4)\n"
        "v_pk_add_f32 v[32:33], v[32:33], v[40:41]\n"
        "v_pk_add_f32 v[34:35], v[34:35], v[42:43]\n"
        "v_pk_add_f32 v[36:37], v[36:37], v[44:45]\n"
        "v_pk_add_f32 v[38:39], v[38:39], v[46:47]\n"
        "s_waitcnt lgkmcnt(2)\n"
        "v_pk_add_f32 v[32:33], v[32:33], v[48:49]\n"
        "v_pk_add_f32 v[34:35], v[34:35], v[50:51]\n"
        "v_pk_add_f32 v[36:37], v[36:37], v[52:53]\n"
        "v_pk_add_f32 v[38:39], v[38:39], v[54:55]\n"
        "s_waitcnt lgkmcnt(0)\n"
        "v_pk_add_f32 v[32:33], v[32:33], v[56:57]\n"
        "v_pk_add_f32 v[34:35], v[34:35], v[58:59]\n"
        "v_pk_add_f32 v[36:37], v[36:37], v[60:61]\n"
        "v_pk_add_f32 v[38:39], v[38:39], v[62:63]\n"
        "v_cvt_pk_bf16_f32 v40, v32, v33\n"
        "v_cvt_pk_bf16_f32 v41, v34, v35\n"
        "v_cvt_pk_bf16_f32 v42, v36, v37\n"
        "v_cvt_pk_bf16_f32 v43, v38, v39\n"
        "buffer_store_dwordx4 v[40:43], v4, s[32:35], s71 offen sc1\n"
        "s_endpgm\n"
        "LSLOW%=:\n"
        "s_waitcnt vmcnt(0) lgkmcnt(0)\n"
        "v_mov_b32 v29, 0xff800000\n"
        "s_mov_b32 s59, 0\n"
        "LS1%=:\n"
        "s_lshr_b32 s57, s59, 3\n"
        "s_lshl_b32 s57, s57, 13\n"
        "s_lshl_b32 s58, s59, 10\n"
        "s_add_u32 s57, s57, s58\n"
        "s_add_u32 s57, s57, s76\n"
        "buffer_load_dwordx4 v[16:19], v124, s[28:31], s57 offen sc1\n"
        "s_waitcnt vmcnt(0)\n"
        "v_mfma_f32_32x32x16_bf16 v[64:79], v[16:19], v[128:131], 0\n"
        "s_nop 12\n"
        "v_max3_f32 v29, v29, v64, v65\n"
        "v_max3_f32 v29, v29, v66, v67\n"
        "v_max3_f32 v29, v29, v68, v69\n"
        "v_max3_f32 v29, v29, v70, v71\n"
        "v_max3_f32 v29, v29, v72, v73\n"
        "v_max3_f32 v29, v29, v74, v75\n"
        "v_max3_f32 v29, v29, v76, v77\n"
        "v_max3_f32 v29, v29, v78, v79\n"
        "s_add_u32 s59, s59, 1\n"
        "s_cmp_lt_u32 s59, 64\n"
        "s_cbranch_scc1 LS1%=\n"
        "v_mov_b32 v30, v29\n"
        "s_nop 1\n"
        "v_permlane32_swap_b32 v29, v30\n"
        "s_nop 1\n"
        "v_min_f32_e64 v80, -v29, -v30\n"
        "v_mov_b32 v0, 0\n"
        "v_min_f32_e64 v81, -v29, -v30\n"
        "v_mov_b32 v1, 0\n"
        "v_min_f32_e64 v82, -v29, -v30\n"
        "v_mov_b32 v2, 0\n"
        "v_min_f32_e64 v83, -v29, -v30\n"
        "v_mov_b32 v3, 0\n"
        "v_min_f32_e64 v84, -v29, -v30\n"
        "v_mov_b32 v4, 0\n"
        "v_min_f32_e64 v85, -v29, -v30\n"
        "v_mov_b32 v5, 0\n"
        "v_min_f32_e64 v86, -v29, -v30\n"
        "v_mov_b32 v6, 0\n"
        "v_min_f32_e64 v87, -v29, -v30\n"
        "v_mov_b32 v7, 0\n"
        "v_min_f32_e64 v88, -v29, -v30\n"
        "v_mov_b32 v8, 0\n"
        "v_min_f32_e64 v89, -v29, -v30\n"
        "v_mov_b32 v9, 0\n"
        "v_min_f32_e64 v90, -v29, -v30\n"
        "v_mov_b32 v10, 0\n"
        "v_min_f32_e64 v91, -v29, -v30\n"
        "v_mov_b32 v11, 0\n"
        "v_min_f32_e64 v92, -v29, -v30\n"
        "v_mov_b32 v12, 0\n"
        "v_min_f32_e64 v93, -v29, -v30\n"
        "v_mov_b32 v13, 0\n"
        "v_min_f32_e64 v94, -v29, -v30\n"
        "v_mov_b32 v14, 0\n"
        "v_min_f32_e64 v95, -v29, -v30\n"
        "v_mov_b32 v15, 0\n"
        "v_mov_b32 v20, 0x3f803f80\n"
        "v_mov_b32 v21, 0x3f803f80\n"
        "v_mov_b32 v22, 0x3f803f80\n"
        "v_mov_b32 v23, 0x3f803f80\n"
        "v_mov_b32 v24, 0x3f803f80\n"
        "v_mov_b32 v25, 0x3f803f80\n"
        "v_mov_b32 v26, 0x3f803f80\n"
        "v_mov_b32 v27, 0x3f803f80\n"
        "v_and_b32 v28, 0xf0, v124\n"
        "v_lshrrev_b32 v29, 1, v124\n"
        "v_and_b32 v29, 0x100, v29\n"
        "v_or_b32 v28, v28, v29\n"
        "v_add_u32 v28, 0x2000, v28\n"
        "s_mov_b32 s59, 0\n"
        "LS2%=:\n"
        "s_lshr_b32 s57, s59, 3\n"
        "s_lshl_b32 s57, s57, 13\n"
        "s_lshl_b32 s58, s59, 10\n"
        "s_add_u32 s57, s57, s58\n"
        "s_add_u32 s57, s57, s76\n"
        "buffer_load_dwordx4 v[16:19], v124, s[28:31], s57 offen sc1\n"
        "s_mov_b32 exec_lo, 0xffff\n"
        "s_mov_b32 exec_hi, 0xffff\n"
        "buffer_load_dwordx4 v[20:23], v28, s[28:31], s57 offen sc1\n"
        "buffer_load_dwordx4 v[24:27], v28, s[28:31], s57 offen offset:512 sc1\n"
        "s_mov_b64 exec, -1\n"
        "s_waitcnt vmcnt(0)\n"
        "v_mfma_f32_32x32x16_bf16 v[64:79], v[16:19], v[128:131], v[80:95]\n"
        "s_nop 12\n"
        "v_exp_f32 v64, v64\n"
        "v_exp_f32 v65, v65\n"
        "v_exp_f32 v66, v66\n"
        "v_exp_f32 v67, v67\n"
        "v_exp_f32 v68, v68\n"
        "v_exp_f32 v69, v69\n"
        "v_exp_f32 v70, v70\n"
        "v_exp_f32 v71, v71\n"
        "v_exp_f32 v72, v72\n"
        "v_exp_f32 v73, v73\n"
        "v_exp_f32 v74, v74\n"
        "v_exp_f32 v75, v75\n"
        "v_exp_f32 v76, v76\n"
        "v_exp_f32 v77, v77\n"
        "v_exp_f32 v78, v78\n"
        "v_exp_f32 v79, v79\n"
        "s_nop 1\n"
        "v_smfmac_f32_32x32x32_bf16 v[0:15], v[20:23], v[64:71], v96\n"
        "v_smfmac_f32_32x32x32_bf16 v[0:15], v[24:27], v[72:79], v96\n"
        "s_nop 12\n"
        "s_add_u32 s59, s59, 1\n"
        "s_cmp_lt_u32 s59, 64\n"
        "s_cbranch_scc1 LS2%=\n"
        "v_rcp_f32 v9, v8\n"
        "s_nop 1\n"
        "s_branch LBACK%=\n"
        ".p2align 12\n"
        "LTB%=:\n"
        "s_waitcnt vmcnt(5) lgkmcnt(0)\n"
        "s_barrier\n"
        "ds_read_b128 v[128:131], v110 offset:0\n"
        "ds_read_b64_tr_b16 v[80:81], v48 offset:0\n"
        "ds_read_b128 v[132:135], v110 offset:2048\n"
        "ds_read_b64_tr_b16 v[82:83], v48 offset:512\n"
        "ds_read_b128 v[64:67], v62 offset:0\n"
        "ds_read_b128 v[68:71], v62 offset:2048\n"
        "ds_read_b64_tr_b16 v[84:85], v48 offset:24576\n"
        "ds_read_b64_tr_b16 v[86:87], v48 offset:25088\n"
        "s_waitcnt lgkmcnt(4)\n"
        "v_mfma_f32_16x16x32_bf16 v[0:3], v[80:83], v[128:131], 0\n"
        "ds_read_b64_tr_b16 v[88:89], v48 offset:32768\n"
        "v_mfma_f32_16x16x32_bf16 v[4:7], v[80:83], v[132:135], 0\n"
        "ds_read_b64_tr_b16 v[90:91], v48 offset:33280\n"
        "s_add_u32 m0, s53, 32768\n"
        "s_xor_b32 s57, s3, 2\n"
        "s_lshl_b32 s58, s57, 7\n"
        "buffer_load_dwordx4 v104, s[4:7], s58 offen lds\n"
        "s_setprio 3\n"
        "s_waitcnt lgkmcnt(5)\n"
        "v_mfma_f32_16x16x32_bf16 v[8:11], v[80:83], v[64:67], 0\n"
        "s_waitcnt lgkmcnt(4)\n"
        "v_mfma_f32_16x16x32_bf16 v[12:15], v[80:83], v[68:71], 0\n"
        "s_add_u32 m0, s53, 40960\n"
        "s_add_u32 s58, s58, 0x10000\n"
        "buffer_load_dwordx4 v104, s[4:7], s58 offen lds\n"
        "s_waitcnt lgkmcnt(2)\n"
        "v_mfma_f32_16x16x32_bf16 v[16:19], v[84:87], v[128:131], 0\n"
        "ds_read_b128 v[136:139], v111 offset:0\n"
        "ds_read_b64_tr_b16 v[92:93], v48 offset:4096\n"
        "v_mfma_f32_16x16x32_bf16 v[20:23], v[84:87], v[132:135], 0\n"
        "ds_read_b128 v[140:143], v111 offset:2048\n"
        "ds_read_b64_tr_b16 v[94:95], v48 offset:4608\n"
        "s_add_u32 m0, s53, 65536\n"
        "s_mul_i32 s58, s57, 196608\n"
        "buffer_load_dwordx4 v105, s[8:11], s58 offen lds\n"
        "v_mfma_f32_16x16x32_bf16 v[24:27], v[84:87], v[64:67], 0\n"
        "ds_read_b128 v[72:75], v63 offset:0\n"
        "v_mfma_f32_16x16x32_bf16 v[28:31], v[84:87], v[68:71], 0\n"
        "ds_read_b128 v[76:79], v63 offset:2048\n"
        "s_add_u32 m0, s53, 106496\n"
        "s_mul_i32 s58, s57, s56\n"
        "buffer_load_dwordx4 v106, s[40:43], s58 offen lds\n"
        "s_waitcnt lgkmcnt(6)\n"
        "v_mfma_f32_16x16x32_bf16 v[32:35], v[88:91], v[128:131], 0\n"
        "ds_read_b64_tr_b16 v[112:113], v48 offset:28672\n"
        "v_mfma_f32_16x16x32_bf16 v[36:39], v[88:91], v[132:135], 0\n"
        "ds_read_b64_tr_b16 v[114:115], v48 offset:29184\n"
        "s_setprio 2\n"
        "v_mfma_f32_16x16x32_bf16 v[40:43], v[88:91], v[64:67], 0\n"
        "v_mfma_f32_16x16x32_bf16 v[44:47], v[88:91], v[68:71], 0\n"
        "s_add_u32 m0, s53, 114688\n"
        "s_nop 0\n"
        "buffer_load_dwordx4 v107, s[44:47], s58 offen lds\n"
        "s_waitcnt lgkmcnt(4)\n"
        "v_mfma_f32_16x16x32_bf16 v[0:3], v[92:95], v[136:139], v[0:3]\n"
        "ds_read_b64_tr_b16 v[80:81], v48 offset:36864\n"
        "v_mfma_f32_16x16x32_bf16 v[4:7], v[92:95], v[140:143], v[4:7]\n"
        "ds_read_b64_tr_b16 v[82:83], v48 offset:37376\n"
        "s_setprio 1\n"
        "s_waitcnt lgkmcnt(4)\n"
        "v_mfma_f32_16x16x32_bf16 v[8:11], v[92:95], v[72:75], v[8:11]\n"
        "v_mfma_f32_16x16x32_bf16 v[12:15], v[92:95], v[76:79], v[12:15]\n"
        "s_setprio 0\n"
        "s_waitcnt lgkmcnt(2)\n"
        "v_mfma_f32_16x16x32_bf16 v[16:19], v[112:115], v[136:139], v[16:19]\n"
        "s_waitcnt vmcnt(5) lgkmcnt(0)\n"
        "s_barrier\n"
        "ds_read_b128 v[160:163], v110 offset:16384\n"
        "ds_read_b64_tr_b16 v[84:85], v48 offset:8192\n"
        "v_mfma_f32_16x16x32_bf16 v[20:23], v[112:115], v[140:143], v[20:23]\n"
        "ds_read_b128 v[164:167], v110 offset:18432\n"
        "ds_read_b64_tr_b16 v[86:87], v48 offset:8704\n"
        "s_setprio 3\n"
        "v_mfma_f32_16x16x32_bf16 v[24:27], v[112:115], v[72:75], v[24:27]\n"
        "ds_read_b128 v[64:67], v62 offset:16384\n"
        "v_mfma_f32_16x16x32_bf16 v[28:31], v[112:115], v[76:79], v[28:31]\n"
        "ds_read_b128 v[68:71], v62 offset:18432\n"
        "s_mov_b32 m0, s53\n"
        "s_xor_b32 s57, s3, 3\n"
        "s_lshl_b32 s58, s57, 7\n"
        "buffer_load_dwordx4 v104, s[4:7], s58 offen lds\n"
        "v_mfma_f32_16x16x32_bf16 v[32:35], v[80:83], v[136:139], v[32:35]\n"
        "ds_read_b64_tr_b16 v[88:89], v48 offset:40960\n"
        "v_mfma_f32_16x16x32_bf16 v[36:39], v[80:83], v[140:143], v[36:39]\n"
        "ds_read_b64_tr_b16 v[90:91], v48 offset:41472\n"
        "s_mul_i32 s92, s48, 0x5851f42d\n"
        "s_mul_hi_u32 s93, s48, 0x4c957f2d\n"
        "s_add_u32 s92, s92, s93\n"
        "v_mfma_f32_16x16x32_bf16 v[40:43], v[80:83], v[72:75], v[40:43]\n"
        "s_mul_i32 s49, s49, 0x4c957f2d\n"
        "s_add_u32 s49, s49, s92\n"
        "s_mul_i32 s48, s48, 0x4c957f2d\n"
        "v_mfma_f32_16x16x32_bf16 v[44:47], v[80:83], v[76:79], v[44:47]\n"
        "s_add_u32 m0, s53, 8192\n"
        "s_add_u32 s58, s58, 0x10000\n"
        "buffer_load_dwordx4 v104, s[4:7], s58 offen lds\n"
        "s_add_u32 s48, s48, 0xf767814f\n"
        "s_addc_u32 s49, s49, 0x14057b7e\n"
        "s_waitcnt lgkmcnt(4)\n"
        "v_mfma_f32_16x16x32_bf16 v[0:3], v[84:87], v[160:163], v[0:3]\n"
        "ds_read_b64_tr_b16 v[92:93], v49 offset:0\n"
        "v_mfma_f32_16x16x32_bf16 v[4:7], v[84:87], v[164:167], v[4:7]\n"
        "ds_read_b64_tr_b16 v[94:95], v49 offset:512\n"
        "s_nop 5\n"
        "s_waitcnt lgkmcnt(4)\n"
        "v_mfma_f32_16x16x32_bf16 v[8:11], v[84:87], v[64:67], v[8:11]\n"
        "v_mfma_f32_16x16x32_bf16 v[12:15], v[84:87], v[68:71], v[12:15]\n"
        "s_nop 5\n"
        "s_add_u32 m0, s53, 49152\n"
        "s_mul_i32 s58, s57, 196608\n"
        "buffer_load_dwordx4 v105, s[8:11], s58 offen lds\n"
        "s_waitcnt lgkmcnt(2)\n"
        "v_mfma_f32_16x16x32_bf16 v[16:19], v[88:91], v[160:163], v[16:19]\n"
        "ds_read_b128 v[168:171], v111 offset:16384\n"
        "ds_read_b64_tr_b16 v[112:113], v48 offset:12288\n"
        "v_mfma_f32_16x16x32_bf16 v[20:23], v[88:91], v[164:167], v[20:23]\n"
        "ds_read_b128 v[172:175], v111 offset:18432\n"
        "ds_read_b64_tr_b16 v[114:115], v48 offset:12800\n"
        "s_nop 5\n"
        "s_setprio 2\n"
        "v_mfma_f32_16x16x32_bf16 v[24:27], v[88:91], v[64:67], v[24:27]\n"
        "ds_read_b128 v[72:75], v63 offset:16384\n"
        "v_mfma_f32_16x16x32_bf16 v[28:31], v[88:91], v[68:71], v[28:31]\n"
        "ds_read_b128 v[76:79], v63 offset:18432\n"
        "s_nop 5\n"
        "s_add_u32 m0, s53, 122880\n"
        "s_mul_i32 s58, s57, s56\n"
        "buffer_load_dwordx4 v106, s[40:43], s58 offen lds\n"
        "s_waitcnt lgkmcnt(6)\n"
        "v_mfma_f32_16x16x32_bf16 v[32:35], v[92:95], v[160:163], v[32:35]\n"
        "ds_read_b64_tr_b16 v[80:81], v48 offset:45056\n"
        "v_mfma_f32_16x16x32_bf16 v[36:39], v[92:95], v[164:167], v[36:39]\n"
        "ds_read_b64_tr_b16 v[82:83], v48 offset:45568\n"
        "s_nop 5\n"
        "v_mfma_f32_16x16x32_bf16 v[40:43], v[92:95], v[64:67], v[40:43]\n"
        "v_mfma_f32_16x16x32_bf16 v[44:47], v[92:95], v[68:71], v[44:47]\n"
        "s_nop 5\n"
        "s_add_u32 m0, s53, 131072\n"
        "s_nop 0\n"
        "buffer_load_dwordx4 v107, s[44:47], s58 offen lds\n"
        "s_setprio 1\n"
        "s_waitcnt lgkmcnt(4)\n"
        "v_mfma_f32_16x16x32_bf16 v[0:3], v[112:115], v[168:171], v[0:3]\n"
        "ds_read_b64_tr_b16 v[84:85], v49 offset:4096\n"
        "v_mfma_f32_16x16x32_bf16 v[4:7], v[112:115], v[172:175], v[4:7]\n"
        "ds_read_b64_tr_b16 v[86:87], v49 offset:4608\n"
        "s_nop 5\n"
        "s_waitcnt lgkmcnt(4)\n"
        "v_mfma_f32_16x16x32_bf16 v[8:11], v[112:115], v[72:75], v[8:11]\n"
        "v_mfma_f32_16x16x32_bf16 v[12:15], v[112:115], v[76:79], v[12:15]\n"
        "s_nop 5\n"
        "s_setprio 0\n"
        "s_waitcnt lgkmcnt(2)\n"
        "v_mfma_f32_16x16x32_bf16 v[16:19], v[80:83], v[168:171], v[16:19]\n"
        "s_waitcnt vmcnt(5) lgkmcnt(0)\n"
        "s_barrier\n"
        "ds_read_b128 v[144:147], v110 offset:32768\n"
        "ds_read_b64_tr_b16 v[88:89], v48 offset:16384\n"
        "v_mfma_f32_16x16x32_bf16 v[20:23], v[80:83], v[172:175], v[20:23]\n"
        "ds_read_b128 v[148:151], v110 offset:34816\n"
        "ds_read_b64_tr_b16 v[90:91], v48 offset:16896\n"
        "s_nop 5\n"
        "s_setprio 3\n"
        "v_mfma_f32_16x16x32_bf16 v[24:27], v[80:83], v[72:75], v[24:27]\n"
        "ds_read_b128 v[64:67], v62 offset:32768\n"
        "v_mfma_f32_16x16x32_bf16 v[28:31], v[80:83], v[76:79], v[28:31]\n"
        "ds_read_b128 v[68:71], v62 offset:34816\n"
        "s_nop 5\n"
        "s_add_u32 m0, s53, 16384\n"
        "s_xor_b32 s57, s3, 4\n"
        "s_lshl_b32 s58, s57, 7\n"
        "buffer_load_dwordx4 v104, s[4:7], s58 offen lds\n"
        "v_mfma_f32_16x16x32_bf16 v[32:35], v[84:87], v[168:171], v[32:35]\n"
        "ds_read_b64_tr_b16 v[92:93], v49 offset:8192\n"
        "v_mfma_f32_16x16x32_bf16 v[36:39], v[84:87], v[172:175], v[36:39]\n"
        "ds_read_b64_tr_b16 v[94:95], v49 offset:8704\n"
        "s_nop 5\n"
        "v_mfma_f32_16x16x32_bf16 v[40:43], v[84:87], v[72:75], v[40:43]\n"
        "v_mfma_f32_16x16x32_bf16 v[44:47], v[84:87], v[76:79], v[44:47]\n"
        "s_nop 5\n"
        "s_add_u32 m0, s53, 24576\n"
        "s_add_u32 s58, s58, 0x10000\n"
        "buffer_load_dwordx4 v104, s[4:7], s58 offen lds\n"
        "s_waitcnt lgkmcnt(4)\n"
        "v_mfma_f32_16x16x32_bf16 v[0:3], v[88:91], v[144:147], v[0:3]\n"
        "ds_read_b64_tr_b16 v[112:113], v49 offset:16384\n"
        "v_mfma_f32_16x16x32_bf16 v[4:7], v[88:91], v[148:151], v[4:7]\n"
        "ds_read_b64_tr_b16 v[114:115], v49 offset:16896\n"
        "s_nop 5\n"
        "s_waitcnt lgkmcnt(4)\n"
        "v_mfma_f32_16x16x32_bf16 v[8:11], v[88:91], v[64:67], v[8:11]\n"
        "v_mfma_f32_16x16x32_bf16 v[12:15], v[88:91], v[68:71], v[12:15]\n"
        "s_nop 5\n"
        "s_add_u32 m0, s53, 57344\n"
        "s_mul_i32 s58, s57, 196608\n"
        "buffer_load_dwordx4 v105, s[8:11], s58 offen lds\n"
        "s_waitcnt lgkmcnt(2)\n"
        "v_mfma_f32_16x16x32_bf16 v[16:19], v[92:95], v[144:147], v[16:19]\n"
        "ds_read_b128 v[152:155], v111 offset:32768\n"
        "ds_read_b64_tr_b16 v[80:81], v48 offset:20480\n"
        "v_mfma_f32_16x16x32_bf16 v[20:23], v[92:95], v[148:151], v[20:23]\n"
        "ds_read_b128 v[156:159], v111 offset:34816\n"
        "ds_read_b64_tr_b16 v[82:83], v48 offset:20992\n"
        "s_nop 5\n"
        "s_setprio 2\n"
        "v_mfma_f32_16x16x32_bf16 v[24:27], v[92:95], v[64:67], v[24:27]\n"
        "ds_read_b128 v[72:75], v63 offset:32768\n"
        "v_mfma_f32_16x16x32_bf16 v[28:31], v[92:95], v[68:71], v[28:31]\n"
        "ds_read_b128 v[76:79], v63 offset:34816\n"
        "s_nop 5\n"
        "s_add_u32 m0, s53, 139264\n"
        "s_mul_i32 s58, s57, s56\n"
        "buffer_load_dwordx4 v106, s[40:43], s58 offen lds\n"
        "s_waitcnt lgkmcnt(6)\n"
        "v_mfma_f32_16x16x32_bf16 v[32:35], v[112:115], v[144:147], v[32:35]\n"
        "ds_read_b64_tr_b16 v[84:85], v49 offset:12288\n"
        "v_mfma_f32_16x16x32_bf16 v[36:39], v[112:115], v[148:151], v[36:39]\n"
        "ds_read_b64_tr_b16 v[86:87], v49 offset:12800\n"
        "s_nop 5\n"
        "v_mfma_f32_16x16x32_bf16 v[40:43], v[112:115], v[64:67], v[40:43]\n"
        "v_mfma_f32_16x16x32_bf16 v[44:47], v[112:115], v[68:71], v[44:47]\n"
        "s_nop 5\n"
        "s_add_u32 m0, s53, 147456\n"
        "s_nop 0\n"
        "buffer_load_dwordx4 v107, s[44:47], s58 offen lds\n"
        "s_setprio 1\n"
        "s_waitcnt lgkmcnt(4)\n"
        "v_mfma_f32_16x16x32_bf16 v[0:3], v[80:83], v[152:155], v[0:3]\n"
        "ds_read_b64_tr_b16 v[88:89], v49 offset:20480\n"
        "v_mfma_f32_16x16x32_bf16 v[4:7], v[80:83], v[156:159], v[4:7]\n"
        "ds_read_b64_tr_b16 v[90:91], v49 offset:20992\n"
        "s_nop 5\n"
        "s_waitcnt lgkmcnt(4)\n"
        "v_mfma_f32_16x16x32_bf16 v[8:11], v[80:83], v[72:75], v[8:11]\n"
        "v_mfma_f32_16x16x32_bf16 v[12:15], v[80:83], v[76:79], v[12:15]\n"
        "s_nop 5\n"
        "s_setprio 0\n"
        "s_waitcnt lgkmcnt(2)\n"
        "v_mfma_f32_16x16x32_bf16 v[16:19], v[84:87], v[152:155], v[16:19]\n"
        "s_waitcnt vmcnt(5) lgkmcnt(0)\n"
        "s_barrier\n"
        "ds_read_b128 v[192:195], v110 offset:0\n"
        "ds_read_b64_tr_b16 v[92:93], v48 offset:0\n"
        "v_mfma_f32_16x16x32_bf16 v[20:23], v[84:87], v[156:159], v[20:23]\n"
        "ds_read_b128 v[196:199], v110 offset:2048\n"
        "ds_read_b64_tr_b16 v[94:95], v48 offset:512\n"
        "s_nop 5\n"
        "s_setprio 3\n"
        "v_mfma_f32_16x16x32_bf16 v[24:27], v[84:87], v[72:75], v[24:27]\n"
        "ds_read_b128 v[64:67], v62 offset:0\n"
        "v_mfma_f32_16x16x32_bf16 v[28:31], v[84:87], v[76:79], v[28:31]\n"
        "ds_read_b128 v[68:71], v62 offset:2048\n"
        "s_nop 5\n"
        "s_add_u32 m0, s53, 32768\n"
        "s_xor_b32 s57, s3, 5\n"
        "s_lshl_b32 s58, s57, 7\n"
        "buffer_load_dwordx4 v104, s[4:7], s58 offen lds\n"
        "v_mfma_f32_16x16x32_bf16 v[32:35], v[88:91], v[152:155], v[32:35]\n"
        "ds_read_b64_tr_b16 v[112:113], v49 offset:24576\n"
        "v_mfma_f32_16x16x32_bf16 v[36:39], v[88:91], v[156:159], v[36:39]\n"
        "ds_read_b64_tr_b16 v[114:115], v49 offset:25088\n"
        "s_nop 5\n"
        "v_mfma_f32_16x16x32_bf16 v[40:43], v[88:91], v[72:75], v[40:43]\n"
        "v_mfma_f32_16x16x32_bf16 v[44:47], v[88:91], v[76:79], v[44:47]\n"
        "s_nop 5\n"
        "s_add_u32 m0, s53, 40960\n"
        "s_add_u32 s58, s58, 0x10000\n"
        "buffer_load_dwordx4 v104, s[4:7], s58 offen lds\n"
        "s_waitcnt lgkmcnt(4)\n"
        "v_mfma_f32_16x16x32_bf16 v[0:3], v[92:95], v[192:195], v[0:3]\n"
        "ds_read_b64_tr_b16 v[80:81], v49 offset:32768\n"
        "v_mfma_f32_16x16x32_bf16 v[4:7], v[92:95], v[196:199], v[4:7]\n"
        "ds_read_b64_tr_b16 v[82:83], v49 offset:33280\n"
        "s_waitcnt lgkmcnt(4)\n"
        "v_mfma_f32_16x16x32_bf16 v[8:11], v[92:95], v[64:67], v[8:11]\n"
        "v_mfma_f32_16x16x32_bf16 v[12:15], v[92:95], v[68:71], v[12:15]\n"
        "s_add_u32 m0, s53, 65536\n"
        "s_mul_i32 s58, s57, 196608\n"
        "buffer_load_dwordx4 v105, s[8:11], s58 offen lds\n"
        "s_waitcnt lgkmcnt(2)\n"
        "v_mfma_f32_16x16x32_bf16 v[16:19], v[112:115], v[192:195], v[16:19]\n"
        "ds_read_b128 v[200:203], v111 offset:0\n"
        "ds_read_b64_tr_b16 v[84:85], v48 offset:4096\n"
        "v_mfma_f32_16x16x32_bf16 v[20:23], v[112:115], v[196:199], v[20:23]\n"
        "ds_read_b128 v[204:207], v111 offset:2048\n"
        "ds_read_b64_tr_b16 v[86:87], v48 offset:4608\n"
        "s_setprio 2\n"
        "v_mfma_f32_16x16x32_bf16 v[24:27], v[112:115], v[64:67], v[24:27]\n"
        "ds_read_b128 v[72:75], v63 offset:0\n"
        "v_mfma_f32_16x16x32_bf16 v[28:31], v[112:115], v[68:71], v[28:31]\n"
        "ds_read_b128 v[76:79], v63 offset:2048\n"
        "s_add_u32 m0, s53, 73728\n"
        "s_mul_i32 s58, s57, s56\n"
        "buffer_load_dwordx4 v106, s[40:43], s58 offen lds\n"
        "s_waitcnt lgkmcnt(6)\n"
        "v_mfma_f32_16x16x32_bf16 v[32:35], v[80:83], v[192:195], v[32:35]\n"
        "ds_read_b64_tr_b16 v[88:89], v49 offset:28672\n"
        "v_mfma_f32_16x16x32_bf16 v[36:39], v[80:83], v[196:199], v[36:39]\n"
        "ds_read_b64_tr_b16 v[90:91], v49 offset:29184\n"
        "v_mfma_f32_16x16x32_bf16 v[40:43], v[80:83], v[64:67], v[40:43]\n"
        "v_mfma_f32_16x16x32_bf16 v[44:47], v[80:83], v[68:71], v[44:47]\n"
        "s_add_u32 m0, s53, 81920\n"
        "s_nop 0\n"
        "buffer_load_dwordx4 v107, s[44:47], s58 offen lds\n"
        "s_setprio 1\n"
        "s_waitcnt lgkmcnt(4)\n"
        "v_mfma_f32_16x16x32_bf16 v[0:3], v[84:87], v[200:203], v[0:3]\n"
        "ds_read_b64_tr_b16 v[92:93], v49 offset:36864\n"
        "v_mfma_f32_16x16x32_bf16 v[4:7], v[84:87], v[204:207], v[4:7]\n"
        "ds_read_b64_tr_b16 v[94:95], v49 offset:37376\n"
        "s_waitcnt lgkmcnt(4)\n"
        "v_mfma_f32_16x16x32_bf16 v[8:11], v[84:87], v[72:75], v[8:11]\n"
        "v_mfma_f32_16x16x32_bf16 v[12:15], v[84:87], v[76:79], v[12:15]\n"
        "s_setprio 0\n"
        "s_waitcnt lgkmcnt(2)\n"
        "v_mfma_f32_16x16x32_bf16 v[16:19], v[88:91], v[200:203], v[16:19]\n"
        "s_waitcnt vmcnt(5) lgkmcnt(0)\n"
        "s_barrier\n"
        "ds_read_b128 v[176:179], v110 offset:16384\n"
        "ds_read_b64_tr_b16 v[112:113], v48 offset:8192\n"
        "v_mfma_f32_16x16x32_bf16 v[20:23], v[88:91], v[204:207], v[20:23]\n"
        "ds_read_b128 v[180:183], v110 offset:18432\n"
        "ds_read_b64_tr_b16 v[114:115], v48 offset:8704\n"
        "s_setprio 3\n"
        "v_mfma_f32_16x16x32_bf16 v[24:27], v[88:91], v[72:75], v[24:27]\n"
        "ds_read_b128 v[64:67], v62 offset:16384\n"
        "v_mfma_f32_16x16x32_bf16 v[28:31], v[88:91], v[76:79], v[28:31]\n"
        "ds_read_b128 v[68:71], v62 offset:18432\n"
        "s_mov_b32 m0, s53\n"
        "s_xor_b32 s57, s3, 6\n"
        "s_lshl_b32 s58, s57, 7\n"
        "buffer_load_dwordx4 v104, s[4:7], s58 offen lds\n"
        "v_mfma_f32_16x16x32_bf16 v[32:35], v[92:95], v[200:203], v[32:35]\n"
        "ds_read_b64_tr_b16 v[80:81], v49 offset:40960\n"
        "v_mfma_f32_16x16x32_bf16 v[36:39], v[92:95], v[204:207], v[36:39]\n"
        "ds_read_b64_tr_b16 v[82:83], v49 offset:41472\n"
        "v_mfma_f32_16x16x32_bf16 v[40:43], v[92:95], v[72:75], v[40:43]\n"
        "v_mfma_f32_16x16x32_bf16 v[44:47], v[92:95], v[76:79], v[44:47]\n"
        "s_add_u32 m0, s53, 8192\n"
        "s_add_u32 s58, s58, 0x10000\n"
        "buffer_load_dwordx4 v104, s[4:7], s58 offen lds\n"
        "s_waitcnt lgkmcnt(4)\n"
        "v_mfma_f32_16x16x32_bf16 v[0:3], v[112:115], v[176:179], v[0:3]\n"
        "ds_read_b64_tr_b16 v[84:85], v49 offset:49152\n"
        "v_mfma_f32_16x16x32_bf16 v[4:7], v[112:115], v[180:183], v[4:7]\n"
        "ds_read_b64_tr_b16 v[86:87], v49 offset:49664\n"
        "s_waitcnt lgkmcnt(4)\n"
        "v_mfma_f32_16x16x32_bf16 v[8:11], v[112:115], v[64:67], v[8:11]\n"
        "v_mfma_f32_16x16x32_bf16 v[12:15], v[112:115], v[68:71], v[12:15]\n"
        "s_add_u32 m0, s53, 49152\n"
        "s_mul_i32 s58, s57, 196608\n"
        "buffer_load_dwordx4 v105, s[8:11], s58 offen lds\n"
        "s_waitcnt lgkmcnt(2)\n"
        "v_mfma_f32_16x16x32_bf16 v[16:19], v[80:83], v[176:179], v[16:19]\n"
        "ds_read_b128 v[184:187], v111 offset:16384\n"
        "ds_read_b64_tr_b16 v[88:89], v48 offset:12288\n"
        "v_mfma_f32_16x16x32_bf16 v[20:23], v[80:83], v[180:183], v[20:23]\n"
        "ds_read_b128 v[188:191], v111 offset:18432\n"
        "ds_read_b64_tr_b16 v[90:91], v48 offset:12800\n"
        "s_setprio 2\n"
        "v_mfma_f32_16x16x32_bf16 v[24:27], v[80:83], v[64:67], v[24:27]\n"
        "ds_read_b128 v[72:75], v63 offset:16384\n"
        "v_mfma_f32_16x16x32_bf16 v[28:31], v[80:83], v[68:71], v[28:31]\n"
        "ds_read_b128 v[76:79], v63 offset:18432\n"
        "s_add_u32 m0, s53, 90112\n"
        "s_mul_i32 s58, s57, s56\n"
        "buffer_load_dwordx4 v106, s[40:43], s58 offen lds\n"
        "s_waitcnt lgkmcnt(6)\n"
        "v_mfma_f32_16x16x32_bf16 v[32:35], v[84:87], v[176:179], v[32:35]\n"
        "ds_read_b64_tr_b16 v[92:93], v49 offset:45056\n"
        "v_mfma_f32_16x16x32_bf16 v[36:39], v[84:87], v[180:183], v[36:39]\n"
        "ds_read_b64_tr_b16 v[94:95], v49 offset:45568\n"
        "v_mfma_f32_16x16x32_bf16 v[40:43], v[84:87], v[64:67], v[40:43]\n"
        "v_mfma_f32_16x16x32_bf16 v[44:47], v[84:87], v[68:71], v[44:47]\n"
        "s_add_u32 m0, s53, 98304\n"
        "s_nop 0\n"
        "buffer_load_dwordx4 v107, s[44:47], s58 offen lds\n"
        "s_setprio 1\n"
        "s_waitcnt lgkmcnt(4)\n"
        "v_mfma_f32_16x16x32_bf16 v[0:3], v[88:91], v[184:187], v[0:3]\n"
        "ds_read_b64_tr_b16 v[112:113], v49 offset:53248\n"
        "v_mfma_f32_16x16x32_bf16 v[4:7], v[88:91], v[188:191], v[4:7]\n"
        "ds_read_b64_tr_b16 v[114:115], v49 offset:53760\n"
        "s_waitcnt lgkmcnt(4)\n"
        "v_mfma_f32_16x16x32_bf16 v[8:11], v[88:91], v[72:75], v[8:11]\n"
        "v_mfma_f32_16x16x32_bf16 v[12:15], v[88:91], v[76:79], v[12:15]\n"
        "s_setprio 0\n"
        "s_waitcnt lgkmcnt(2)\n"
        "v_mfma_f32_16x16x32_bf16 v[16:19], v[92:95], v[184:187], v[16:19]\n"
        "s_waitcnt vmcnt(5) lgkmcnt(0)\n"
        "s_barrier\n"
        "ds_read_b128 v[224:227], v110 offset:32768\n"
        "ds_read_b64_tr_b16 v[80:81], v48 offset:16384\n"
        "v_mfma_f32_16x16x32_bf16 v[20:23], v[92:95], v[188:191], v[20:23]\n"
        "ds_read_b128 v[228:231], v110 offset:34816\n"
        "ds_read_b64_tr_b16 v[82:83], v48 offset:16896\n"
        "s_setprio 3\n"
        "v_mfma_f32_16x16x32_bf16 v[24:27], v[92:95], v[72:75], v[24:27]\n"
        "ds_read_b128 v[64:67], v62 offset:32768\n"
        "v_mfma_f32_16x16x32_bf16 v[28:31], v[92:95], v[76:79], v[28:31]\n"
        "ds_read_b128 v[68:71], v62 offset:34816\n"
        "s_add_u32 m0, s53, 16384\n"
        "s_xor_b32 s57, s3, 7\n"
        "s_lshl_b32 s58, s57, 7\n"
        "buffer_load_dwordx4 v104, s[4:7], s58 offen lds\n"
        "v_mfma_f32_16x16x32_bf16 v[32:35], v[112:115], v[184:187], v[32:35]\n"
        "ds_read_b64_tr_b16 v[84:85], v48 offset:24576\n"
        "v_mfma_f32_16x16x32_bf16 v[36:39], v[112:115], v[188:191], v[36:39]\n"
        "ds_read_b64_tr_b16 v[86:87], v48 offset:25088\n"
        "v_mfma_f32_16x16x32_bf16 v[40:43], v[112:115], v[72:75], v[40:43]\n"
        "v_mfma_f32_16x16x32_bf16 v[44:47], v[112:115], v[76:79], v[44:47]\n"
        "s_add_u32 m0, s53, 24576\n"
        "s_add_u32 s58, s58, 0x10000\n"
        "buffer_load_dwordx4 v104, s[4:7], s58 offen lds\n"
        "s_waitcnt lgkmcnt(4)\n"
        "v_mfma_f32_16x16x32_bf16 v[0:3], v[80:83], v[224:227], v[0:3]\n"
        "ds_read_b64_tr_b16 v[88:89], v48 offset:32768\n"
        "v_mfma_f32_16x16x32_bf16 v[4:7], v[80:83], v[228:231], v[4:7]\n"
        "ds_read_b64_tr_b16 v[90:91], v48 offset:33280\n"
        "s_waitcnt lgkmcnt(4)\n"
        "v_mfma_f32_16x16x32_bf16 v[8:11], v[80:83], v[64:67], v[8:11]\n"
        "v_mfma_f32_16x16x32_bf16 v[12:15], v[80:83], v[68:71], v[12:15]\n"
        "s_add_u32 m0, s53, 57344\n"
        "s_mul_i32 s58, s57, 196608\n"
        "buffer_load_dwordx4 v105, s[8:11], s58 offen lds\n"
        "s_waitcnt lgkmcnt(2)\n"
        "v_mfma_f32_16x16x32_bf16 v[16:19], v[84:87], v[224:227], v[16:19]\n"
        "ds_read_b128 v[232:235], v111 offset:32768\n"
        "ds_read_b64_tr_b16 v[92:93], v48 offset:20480\n"
        "v_mfma_f32_16x16x32_bf16 v[20:23], v[84:87], v[228:231], v[20:23]\n"
        "ds_read_b128 v[236:239], v111 offset:34816\n"
        "ds_read_b64_tr_b16 v[94:95], v48 offset:20992\n"
        "s_setprio 2\n"
        "v_mfma_f32_16x16x32_bf16 v[24:27], v[84:87], v[64:67], v[24:27]\n"
        "ds_read_b128 v[72:75], v63 offset:32768\n"
        "v_mfma_f32_16x16x32_bf16 v[28:31], v[84:87], v[68:71], v[28:31]\n"
        "ds_read_b128 v[76:79], v63 offset:34816\n"
        "s_add_u32 m0, s53, 106496\n"
        "s_mul_i32 s58, s57, s56\n"
        "buffer_load_dwordx4 v106, s[40:43], s58 offen lds\n"
        "s_waitcnt lgkmcnt(6)\n"
        "v_mfma_f32_16x16x32_bf16 v[32:35], v[88:91], v[224:227], v[32:35]\n"
        "ds_read_b64_tr_b16 v[112:113], v48 offset:28672\n"
        "v_mfma_f32_16x16x32_bf16 v[36:39], v[88:91], v[228:231], v[36:39]\n"
        "ds_read_b64_tr_b16 v[114:115], v48 offset:29184\n"
        "v_mfma_f32_16x16x32_bf16 v[40:43], v[88:91], v[64:67], v[40:43]\n"
        "v_mfma_f32_16x16x32_bf16 v[44:47], v[88:91], v[68:71], v[44:47]\n"
        "s_add_u32 m0, s53, 114688\n"
        "s_nop 0\n"
        "buffer_load_dwordx4 v107, s[44:47], s58 offen lds\n"
        "s_setprio 1\n"
        "s_waitcnt lgkmcnt(4)\n"
        "v_mfma_f32_16x16x32_bf16 v[0:3], v[92:95], v[232:235], v[0:3]\n"
        "ds_read_b64_tr_b16 v[80:81], v48 offset:36864\n"
        "v_mfma_f32_16x16x32_bf16 v[4:7], v[92:95], v[236:239], v[4:7]\n"
        "ds_read_b64_tr_b16 v[82:83], v48 offset:37376\n"
        "v_lshrrev_b32 v50, 7, v124\n"
        "s_lshl_b32 s59, s80, 3\n"
        "s_add_u32 s59, s59, s52\n"
        "s_mul_i32 s59, s59, 28672\n"
        "s_lshl_b32 s60, s82, 7\n"
        "s_add_u32 s59, s59, s60\n"
        "v_mul_u32_u24 v50, 0xe00, v50\n"
        "buffer_load_dword v51, v50, s[12:15], s59 offen\n"
        "buffer_load_dword v51, v50, s[16:19], s59 offen\n"
        "s_waitcnt lgkmcnt(4)\n"
        "v_mfma_f32_16x16x32_bf16 v[8:11], v[92:95], v[72:75], v[8:11]\n"
        "v_mfma_f32_16x16x32_bf16 v[12:15], v[92:95], v[76:79], v[12:15]\n"
        "s_setprio 0\n"
        "s_waitcnt lgkmcnt(2)\n"
        "v_mfma_f32_16x16x32_bf16 v[16:19], v[112:115], v[232:235], v[16:19]\n"
        "s_waitcnt vmcnt(7) lgkmcnt(0)\n"
        "s_barrier\n"
        "ds_read_b128 v[208:211], v110 offset:0\n"
        "ds_read_b64_tr_b16 v[84:85], v48 offset:0\n"
        "v_mfma_f32_16x16x32_bf16 v[20:23], v[112:115], v[236:239], v[20:23]\n"
        "ds_read_b128 v[212:215], v110 offset:2048\n"
        "ds_read_b64_tr_b16 v[86:87], v48 offset:512\n"
        "s_setprio 3\n"
        "v_mfma_f32_16x16x32_bf16 v[24:27], v[112:115], v[72:75], v[24:27]\n"
        "ds_read_b128 v[64:67], v62 offset:0\n"
        "v_mfma_f32_16x16x32_bf16 v[28:31], v[112:115], v[76:79], v[28:31]\n"
        "ds_read_b128 v[68:71], v62 offset:2048\n"
        "s_add_u32 m0, s53, 122880\n"
        "s_xor_b32 s57, s3, 0\n"
        "s_mul_i32 s58, s57, 229376\n"
        "buffer_load_dwordx4 v108, s[12:15], s58 offen lds\n"
        "v_mfma_f32_16x16x32_bf16 v[32:35], v[80:83], v[232:235], v[32:35]\n"
        "ds_read_b64_tr_b16 v[88:89], v48 offset:40960\n"
        "v_mfma_f32_16x16x32_bf16 v[36:39], v[80:83], v[236:239], v[36:39]\n"
        "ds_read_b64_tr_b16 v[90:91], v48 offset:41472\n"
        "v_mfma_f32_16x16x32_bf16 v[40:43], v[80:83], v[72:75], v[40:43]\n"
        "v_mfma_f32_16x16x32_bf16 v[44:47], v[80:83], v[76:79], v[44:47]\n"
        "s_add_u32 m0, s53, 131072\n"
        "s_nop 0\n"
        "buffer_load_dwordx4 v108, s[16:19], s58 offen lds\n"
        "s_waitcnt lgkmcnt(4)\n"
        "v_mfma_f32_16x16x32_bf16 v[0:3], v[84:87], v[208:211], v[0:3]\n"
        "ds_read_b64_tr_b16 v[92:93], v49 offset:0\n"
        "v_mfma_f32_16x16x32_bf16 v[4:7], v[84:87], v[212:215], v[4:7]\n"
        "ds_read_b64_tr_b16 v[94:95], v49 offset:512\n"
        "s_waitcnt lgkmcnt(4)\n"
        "v_mfma_f32_16x16x32_bf16 v[8:11], v[84:87], v[64:67], v[8:11]\n"
        "v_mfma_f32_16x16x32_bf16 v[12:15], v[84:87], v[68:71], v[12:15]\n"
        "s_add_u32 m0, s53, 139264\n"
        "s_xor_b32 s57, s3, 2\n"
        "s_mul_i32 s58, s57, 229376\n"
        "buffer_load_dwordx4 v108, s[12:15], s58 offen lds\n"
        "s_waitcnt lgkmcnt(2)\n"
        "v_mfma_f32_16x16x32_bf16 v[16:19], v[88:91], v[208:211], v[16:19]\n"
        "ds_read_b128 v[216:219], v111 offset:0\n"
        "ds_read_b64_tr_b16 v[112:113], v48 offset:4096\n"
        "v_mfma_f32_16x16x32_bf16 v[20:23], v[88:91], v[212:215], v[20:23]\n"
        "ds_read_b128 v[220:223], v111 offset:2048\n"
        "ds_read_b64_tr_b16 v[114:115], v48 offset:4608\n"
        "s_setprio 2\n"
        "v_mfma_f32_16x16x32_bf16 v[24:27], v[88:91], v[64:67], v[24:27]\n"
        "ds_read_b128 v[72:75], v63 offset:0\n"
        "v_mfma_f32_16x16x32_bf16 v[28:31], v[88:91], v[68:71], v[28:31]\n"
        "ds_read_b128 v[76:79], v63 offset:2048\n"
        "s_add_u32 m0, s53, 147456\n"
        "s_nop 0\n"
        "buffer_load_dwordx4 v108, s[16:19], s58 offen lds\n"
        "s_waitcnt lgkmcnt(6)\n"
        "v_mfma_f32_16x16x32_bf16 v[32:35], v[92:95], v[208:211], v[32:35]\n"
        "ds_read_b64_tr_b16 v[80:81], v48 offset:45056\n"
        "v_mfma_f32_16x16x32_bf16 v[36:39], v[92:95], v[212:215], v[36:39]\n"
        "ds_read_b64_tr_b16 v[82:83], v48 offset:45568\n"
        "v_mfma_f32_16x16x32_bf16 v[40:43], v[92:95], v[64:67], v[40:43]\n"
        "v_mfma_f32_16x16x32_bf16 v[44:47], v[92:95], v[68:71], v[44:47]\n"
        "s_add_u32 m0, s53, 73728\n"
        "s_xor_b32 s57, s3, 3\n"
        "s_mul_i32 s58, s57, 229376\n"
        "buffer_load_dwordx4 v108, s[12:15], s58 offen lds\n"
        "s_setprio 1\n"
        "s_waitcnt lgkmcnt(4)\n"
        "v_mfma_f32_16x16x32_bf16 v[0:3], v[112:115], v[216:219], v[0:3]\n"
        "ds_read_b64_tr_b16 v[84:85], v49 offset:4096\n"
        "v_mfma_f32_16x16x32_bf16 v[4:7], v[112:115], v[220:223], v[4:7]\n"
        "ds_read_b64_tr_b16 v[86:87], v49 offset:4608\n"
        "s_waitcnt lgkmcnt(4)\n"
        "v_mfma_f32_16x16x32_bf16 v[8:11], v[112:115], v[72:75], v[8:11]\n"
        "v_mfma_f32_16x16x32_bf16 v[12:15], v[112:115], v[76:79], v[12:15]\n"
        "s_add_u32 m0, s53, 81920\n"
        "s_nop 0\n"
        "buffer_load_dwordx4 v108, s[16:19], s58 offen lds\n"
        "s_setprio 0\n"
        "s_waitcnt lgkmcnt(2)\n"
        "v_mfma_f32_16x16x32_bf16 v[16:19], v[80:83], v[216:219], v[16:19]\n"
        "s_waitcnt vmcnt(8) lgkmcnt(0)\n"
        "s_barrier\n"
        "ds_read_b128 v[240:243], v110 offset:16384\n"
        "ds_read_b64_tr_b16 v[88:89], v48 offset:8192\n"
        "v_mfma_f32_16x16x32_bf16 v[20:23], v[80:83], v[220:223], v[20:23]\n"
        "ds_read_b128 v[244:247], v110 offset:18432\n"
        "ds_read_b64_tr_b16 v[90:91], v48 offset:8704\n"
        "s_setprio 3\n"
        "v_mfma_f32_16x16x32_bf16 v[24:27], v[80:83], v[72:75], v[24:27]\n"
        "ds_read_b128 v[64:67], v62 offset:16384\n"
        "v_mfma_f32_16x16x32_bf16 v[28:31], v[80:83], v[76:79], v[28:31]\n"
        "ds_read_b128 v[68:71], v62 offset:18432\n"
        "s_mov_b32 m0, s53\n"
        "s_xor_b32 s57, s3, 1\n"
        "s_mul_i32 s58, s57, 229376\n"
        "buffer_load_dwordx4 v108, s[12:15], s58 offen lds\n"
        "v_mfma_f32_16x16x32_bf16 v[32:35], v[84:87], v[216:219], v[32:35]\n"
        "ds_read_b128 v[248:251], v111 offset:16384\n"
        "ds_read_b64_tr_b16 v[92:93], v48 offset:12288\n"
        "v_mfma_f32_16x16x32_bf16 v[36:39], v[84:87], v[220:223], v[36:39]\n"
        "ds_read_b128 v[252:255], v111 offset:18432\n"
        "ds_read_b64_tr_b16 v[94:95], v48 offset:12800\n"
        "v_mfma_f32_16x16x32_bf16 v[40:43], v[84:87], v[72:75], v[40:43]\n"
        "ds_read_b128 v[72:75], v63 offset:16384\n"
        "v_mfma_f32_16x16x32_bf16 v[44:47], v[84:87], v[76:79], v[44:47]\n"
        "ds_read_b128 v[76:79], v63 offset:18432\n"
        "s_add_u32 m0, s53, 8192\n"
        "s_nop 0\n"
        "buffer_load_dwordx4 v108, s[16:19], s58 offen lds\n"
        "s_waitcnt lgkmcnt(8)\n"
        "v_mfma_f32_16x16x32_bf16 v[0:3], v[88:91], v[240:243], v[0:3]\n"
        "ds_read_b64_tr_b16 v[112:113], v49 offset:8192\n"
        "v_mfma_f32_16x16x32_bf16 v[4:7], v[88:91], v[244:247], v[4:7]\n"
        "ds_read_b64_tr_b16 v[114:115], v49 offset:8704\n"
        "s_waitcnt lgkmcnt(8)\n"
        "v_mfma_f32_16x16x32_bf16 v[8:11], v[88:91], v[64:67], v[8:11]\n"
        "v_mfma_f32_16x16x32_bf16 v[12:15], v[88:91], v[68:71], v[12:15]\n"
        "s_add_u32 m0, s53, 90112\n"
        "s_xor_b32 s57, s3, 4\n"
        "s_mul_i32 s58, s57, 229376\n"
        "buffer_load_dwordx4 v108, s[12:15], s58 offen lds\n"
        "s_waitcnt lgkmcnt(4)\n"
        "v_mfma_f32_16x16x32_bf16 v[0:3], v[92:95], v[248:251], v[0:3]\n"
        "ds_read_b64_tr_b16 v[80:81], v49 offset:12288\n"
        "v_mfma_f32_16x16x32_bf16 v[4:7], v[92:95], v[252:255], v[4:7]\n"
        "ds_read_b64_tr_b16 v[82:83], v49 offset:12800\n"
        "s_setprio 2\n"
        "s_mov_b32 s40, s65\n"
        "s_waitcnt lgkmcnt(4)\n"
        "v_mfma_f32_16x16x32_bf16 v[8:11], v[92:95], v[72:75], v[8:11]\n"
        "v_mfma_f32_16x16x32_bf16 v[12:15], v[92:95], v[76:79], v[12:15]\n"
        "s_add_u32 m0, s53, 98304\n"
        "s_nop 0\n"
        "buffer_load_dwordx4 v108, s[16:19], s58 offen lds\n"
        "s_waitcnt lgkmcnt(2)\n"
        "v_mfma_f32_16x16x32_bf16 v[16:19], v[112:115], v[240:243], v[16:19]\n"
        "ds_read_b64_tr_b16 v[84:85], v49 offset:16384\n"
        "v_mfma_f32_16x16x32_bf16 v[20:23], v[112:115], v[244:247], v[20:23]\n"
        "ds_read_b64_tr_b16 v[86:87], v49 offset:16896\n"
        "v_mfma_f32_16x16x32_bf16 v[24:27], v[112:115], v[64:67], v[24:27]\n"
        "v_mfma_f32_16x16x32_bf16 v[28:31], v[112:115], v[68:71], v[28:31]\n"
        "s_add_u32 m0, s53, 32768\n"
        "s_xor_b32 s57, s3, 6\n"
        "s_mul_i32 s58, s57, 229376\n"
        "buffer_load_dwordx4 v108, s[12:15], s58 offen lds\n"
        "s_setprio 1\n"
        "s_waitcnt lgkmcnt(2)\n"
        "v_mfma_f32_16x16x32_bf16 v[16:19], v[80:83], v[248:251], v[16:19]\n"
        "ds_read_b64_tr_b16 v[88:89], v49 offset:20480\n"
        "v_mfma_f32_16x16x32_bf16 v[20:23], v[80:83], v[252:255], v[20:23]\n"
        "ds_read_b64_tr_b16 v[90:91], v49 offset:20992\n"
        "v_pk_mul_f32 v[0:1], v[0:1], s[40:41] op_sel_hi:[1,0]\n"
        "v_pk_mul_f32 v[2:3], v[2:3], s[40:41] op_sel_hi:[1,0]\n"
        "v_pk_mul_f32 v[4:5], v[4:5], s[40:41] op_sel_hi:[1,0]\n"
        "v_pk_mul_f32 v[6:7], v[6:7], s[40:41] op_sel_hi:[1,0]\n"
        "v_mfma_f32_16x16x32_bf16 v[24:27], v[80:83], v[72:75], v[24:27]\n"
        "v_cvt_pk_bf16_f32 v0, v0, v1\n"
        "v_cvt_pk_bf16_f32 v1, v2, v3\n"
        "v_cvt_pk_bf16_f32 v2, v4, v5\n"
        "v_cvt_pk_bf16_f32 v3, v6, v7\n"
        "v_mfma_f32_16x16x32_bf16 v[28:31], v[80:83], v[76:79], v[28:31]\n"
        "s_add_u32 m0, s53, 40960\n"
        "s_nop 0\n"
        "buffer_load_dwordx4 v108, s[16:19], s58 offen lds\n"
        "s_setprio 0\n"
        "s_nop 0\n"
        "v_permlane16_swap_b32 v0, v2\n"
        "v_permlane16_swap_b32 v1, v3\n"
        "s_waitcnt lgkmcnt(2)\n"
        "v_mfma_f32_16x16x32_bf16 v[32:35], v[84:87], v[240:243], v[32:35]\n"
        "s_waitcnt vmcnt(4) lgkmcnt(0)\n"
        "s_barrier\n"
        "ds_read_b64_tr_b16 v[92:93], v120 offset:24576\n"
        "buffer_store_dwordx4 v[0:3], v124, s[28:31], s61 offen sc1\n"
        "v_mfma_f32_16x16x32_bf16 v[36:39], v[84:87], v[244:247], v[36:39]\n"
        "ds_read_b64_tr_b16 v[94:95], v120 offset:25088\n"
        "s_setprio 3\n"
        "v_pk_mul_f32 v[8:9], v[8:9], s[40:41] op_sel_hi:[1,0]\n"
        "v_pk_mul_f32 v[10:11], v[10:11], s[40:41] op_sel_hi:[1,0]\n"
        "v_pk_mul_f32 v[12:13], v[12:13], s[40:41] op_sel_hi:[1,0]\n"
        "v_pk_mul_f32 v[14:15], v[14:15], s[40:41] op_sel_hi:[1,0]\n"
        "v_mfma_f32_16x16x32_bf16 v[40:43], v[84:87], v[64:67], v[40:43]\n"
        "ds_read_b64_tr_b16 v[112:113], v121 offset:24576\n"
        "v_cvt_pk_bf16_f32 v4, v8, v9\n"
        "v_cvt_pk_bf16_f32 v5, v10, v11\n"
        "v_cvt_pk_bf16_f32 v6, v12, v13\n"
        "v_cvt_pk_bf16_f32 v7, v14, v15\n"
        "v_mfma_f32_16x16x32_bf16 v[44:47], v[84:87], v[68:71], v[44:47]\n"
        "ds_read_b64_tr_b16 v[114:115], v121 offset:25088\n"
        "s_nop 0\n"
        "v_permlane16_swap_b32 v4, v6\n"
        "v_permlane16_swap_b32 v5, v7\n"
        "v_mfma_f32_16x16x32_bf16 v[32:35], v[88:91], v[248:251], v[32:35]\n"
        "ds_read_b64_tr_b16 v[80:81], v120 offset:32768\n"
        "buffer_store_dwordx4 v[4:7], v124, s[28:31], s62 offen sc1\n"
        "v_mfma_f32_16x16x32_bf16 v[36:39], v[88:91], v[252:255], v[36:39]\n"
        "ds_read_b64_tr_b16 v[82:83], v120 offset:33280\n"
        "v_mfma_f32_16x16x32_bf16 v[40:43], v[88:91], v[72:75], v[40:43]\n"
        "ds_read_b64_tr_b16 v[84:85], v121 offset:32768\n"
        "v_mfma_f32_16x16x32_bf16 v[44:47], v[88:91], v[76:79], v[44:47]\n"
        "ds_read_b64_tr_b16 v[86:87], v121 offset:33280\n"
        "s_waitcnt lgkmcnt(4)\n"
        "v_mfma_f32_16x16x32_bf16 v[48:51], v[92:95], v[128:131], 0\n"
        "ds_read_b64_tr_b16 v[88:89], v120 offset:28672\n"
        "v_mfma_f32_16x16x32_bf16 v[52:55], v[92:95], v[132:135], 0\n"
        "ds_read_b64_tr_b16 v[90:91], v120 offset:29184\n"
        "v_mfma_f32_16x16x32_bf16 v[64:67], v[112:115], v[128:131], 0\n"
        "ds_read_b64_tr_b16 v[92:93], v121 offset:28672\n"
        "v_mfma_f32_16x16x32_bf16 v[68:71], v[112:115], v[132:135], 0\n"
        "ds_read_b64_tr_b16 v[94:95], v121 offset:29184\n"
        "v_mul_f32 v8, v16, v16\n"
        "v_mul_f32 v9, v17, v17\n"
        "v_mul_f32 v10, v18, v18\n"
        "v_mul_f32 v11, v19, v19\n"
        "s_waitcnt lgkmcnt(4)\n"
        "v_mfma_f32_16x16x32_bf16 v[56:59], v[80:83], v[128:131], 0\n"
        "ds_read_b64_tr_b16 v[112:113], v120 offset:36864\n"
        "v_fma_f32 v8, v8, s50, v122\n"
        "v_fma_f32 v9, v9, s50, v122\n"
        "v_fma_f32 v10, v10, s50, v122\n"
        "v_fma_f32 v11, v11, s50, v122\n"
        "v_mfma_f32_16x16x32_bf16 v[60:63], v[80:83], v[132:135], 0\n"
        "ds_read_b64_tr_b16 v[114:115], v120 offset:37376\n"
        "v_mul_f32 v8, v8, v16\n"
        "v_mul_f32 v9, v9, v17\n"
        "v_mul_f32 v10, v10, v18\n"
        "v_mul_f32 v11, v11, v19\n"
        "v_mfma_f32_16x16x32_bf16 v[72:75], v[84:87], v[128:131], 0\n"
        "ds_read_b64_tr_b16 v[80:81], v121 offset:36864\n"
        "v_exp_f32 v8, v8\n"
        "v_exp_f32 v9, v9\n"
        "v_mfma_f32_16x16x32_bf16 v[76:79], v[84:87], v[132:135], 0\n"
        "ds_read_b64_tr_b16 v[82:83], v121 offset:37376\n"
        "v_exp_f32 v10, v10\n"
        "v_exp_f32 v11, v11\n"
        "s_waitcnt lgkmcnt(4)\n"
        "v_mfma_f32_16x16x32_bf16 v[48:51], v[88:91], v[136:139], v[48:51]\n"
        "ds_read_b64_tr_b16 v[84:85], v120 offset:40960\n"
        "v_mul_f32 v32, v16, v32\n"
        "v_mul_f32 v33, v17, v33\n"
        "v_mul_f32 v34, v18, v34\n"
        "v_mul_f32 v35, v19, v35\n"
        "v_mfma_f32_16x16x32_bf16 v[52:55], v[88:91], v[140:143], v[52:55]\n"
        "ds_read_b64_tr_b16 v[86:87], v120 offset:41472\n"
        "v_add_f32 v8, 1.0, v8\n"
        "v_add_f32 v9, 1.0, v9\n"
        "v_add_f32 v10, 1.0, v10\n"
        "v_add_f32 v11, 1.0, v11\n"
        "v_mfma_f32_16x16x32_bf16 v[64:67], v[92:95], v[136:139], v[64:67]\n"
        "ds_read_b64_tr_b16 v[88:89], v121 offset:40960\n"
        "v_rcp_f32 v8, v8\n"
        "v_rcp_f32 v9, v9\n"
        "v_mfma_f32_16x16x32_bf16 v[68:71], v[92:95], v[140:143], v[68:71]\n"
        "ds_read_b64_tr_b16 v[90:91], v121 offset:41472\n"
        "v_rcp_f32 v10, v10\n"
        "v_rcp_f32 v11, v11\n"
        "s_waitcnt lgkmcnt(4)\n"
        "v_mfma_f32_16x16x32_bf16 v[56:59], v[112:115], v[136:139], v[56:59]\n"
        "ds_read_b64_tr_b16 v[92:93], v120 offset:49152\n"
        "v_mul_f32 v32, v32, v8\n"
        "v_mul_f32 v33, v33, v9\n"
        "v_mul_f32 v34, v34, v10\n"
        "v_mul_f32 v35, v35, v11\n"
        "v_mfma_f32_16x16x32_bf16 v[60:63], v[112:115], v[140:143], v[60:63]\n"
        "ds_read_b64_tr_b16 v[94:95], v120 offset:49664\n"
        "v_mul_f32 v8, v20, v20\n"
        "v_mul_f32 v9, v21, v21\n"
        "v_mul_f32 v10, v22, v22\n"
        "v_mul_f32 v11, v23, v23\n"
        "v_mfma_f32_16x16x32_bf16 v[72:75], v[80:83], v[136:139], v[72:75]\n"
        "ds_read_b64_tr_b16 v[112:113], v121 offset:49152\n"
        "v_fma_f32 v8, v8, s50, v122\n"
        "v_fma_f32 v9, v9, s50, v122\n"
        "v_fma_f32 v10, v10, s50, v122\n"
        "v_fma_f32 v11, v11, s50, v122\n"
        "v_mfma_f32_16x16x32_bf16 v[76:79], v[80:83], v[140:143], v[76:79]\n"
        "ds_read_b64_tr_b16 v[114:115], v121 offset:49664\n"
        "v_mul_f32 v8, v8, v20\n"
        "v_mul_f32 v9, v9, v21\n"
        "v_mul_f32 v10, v10, v22\n"
        "v_mul_f32 v11, v11, v23\n"
        "s_waitcnt lgkmcnt(4)\n"
        "v_mfma_f32_16x16x32_bf16 v[48:51], v[84:87], v[144:147], v[48:51]\n"
        "ds_read_b64_tr_b16 v[80:81], v120 offset:45056\n"
        "v_exp_f32 v8, v8\n"
        "v_exp_f32 v9, v9\n"
        "v_mfma_f32_16x16x32_bf16 v[52:55], v[84:87], v[148:151], v[52:55]\n"
        "ds_read_b64_tr_b16 v[82:83], v120 offset:45568\n"
        "s_setprio 2\n"
        "v_exp_f32 v10, v10\n"
        "v_exp_f32 v11, v11\n"
        "v_mfma_f32_16x16x32_bf16 v[64:67], v[88:91], v[144:147], v[64:67]\n"
        "ds_read_b64_tr_b16 v[84:85], v121 offset:45056\n"
        "v_mul_f32 v36, v20, v36\n"
        "v_mul_f32 v37, v21, v37\n"
        "v_mul_f32 v38, v22, v38\n"
        "v_mul_f32 v39, v23, v39\n"
        "v_mfma_f32_16x16x32_bf16 v[68:71], v[88:91], v[148:151], v[68:71]\n"
        "ds_read_b64_tr_b16 v[86:87], v121 offset:45568\n"
        "v_add_f32 v8, 1.0, v8\n"
        "v_add_f32 v9, 1.0, v9\n"
        "v_add_f32 v10, 1.0, v10\n"
        "v_add_f32 v11, 1.0, v11\n"
        "s_waitcnt lgkmcnt(4)\n"
        "v_mfma_f32_16x16x32_bf16 v[56:59], v[92:95], v[144:147], v[56:59]\n"
        "ds_read_b64_tr_b16 v[88:89], v120 offset:53248\n"
        "v_rcp_f32 v8, v8\n"
        "v_rcp_f32 v9, v9\n"
        "v_mfma_f32_16x16x32_bf16 v[60:63], v[92:95], v[148:151], v[60:63]\n"
        "ds_read_b64_tr_b16 v[90:91], v120 offset:53760\n"
        "v_rcp_f32 v10, v10\n"
        "v_rcp_f32 v11, v11\n"
        "v_mfma_f32_16x16x32_bf16 v[72:75], v[112:115], v[144:147], v[72:75]\n"
        "ds_read_b64_tr_b16 v[92:93], v121 offset:53248\n"
        "v_mul_f32 v36, v36, v8\n"
        "v_mul_f32 v37, v37, v9\n"
        "v_mul_f32 v38, v38, v10\n"
        "v_mul_f32 v39, v39, v11\n"
        "v_mfma_f32_16x16x32_bf16 v[76:79], v[112:115], v[148:151], v[76:79]\n"
        "ds_read_b64_tr_b16 v[94:95], v121 offset:53760\n"
        "s_waitcnt lgkmcnt(4)\n"
        "v_mfma_f32_16x16x32_bf16 v[48:51], v[80:83], v[152:155], v[48:51]\n"
        "ds_read_b64_tr_b16 v[112:113], v116 offset:0\n"
        "v_mfma_f32_16x16x32_bf16 v[52:55], v[80:83], v[156:159], v[52:55]\n"
        "ds_read_b64_tr_b16 v[114:115], v116 offset:512\n"
        "v_cvt_pk_bf16_f32 v144, v32, v33\n"
        "v_cvt_pk_bf16_f32 v145, v34, v35\n"
        "v_cvt_pk_bf16_f32 v146, v36, v37\n"
        "v_cvt_pk_bf16_f32 v147, v38, v39\n"
        "v_mfma_f32_16x16x32_bf16 v[64:67], v[84:87], v[152:155], v[64:67]\n"
        "ds_read_b64_tr_b16 v[80:81], v117 offset:0\n"
        "s_nop 0\n"
        "v_permlane16_swap_b32 v144, v146\n"
        "v_permlane16_swap_b32 v145, v147\n"
        "v_mfma_f32_16x16x32_bf16 v[68:71], v[84:87], v[156:159], v[68:71]\n"
        "ds_read_b64_tr_b16 v[82:83], v117 offset:512\n"
        "v_mul_f32 v8, v24, v24\n"
        "v_mul_f32 v9, v25, v25\n"
        "v_mul_f32 v10, v26, v26\n"
        "v_mul_f32 v11, v27, v27\n"
        "s_waitcnt lgkmcnt(4)\n"
        "v_mfma_f32_16x16x32_bf16 v[56:59], v[88:91], v[152:155], v[56:59]\n"
        "ds_read_b64_tr_b16 v[84:85], v116 offset:8192\n"
        "v_fma_f32 v8, v8, s50, v122\n"
        "v_fma_f32 v9, v9, s50, v122\n"
        "v_fma_f32 v10, v10, s50, v122\n"
        "v_fma_f32 v11, v11, s50, v122\n"
        "v_mfma_f32_16x16x32_bf16 v[60:63], v[88:91], v[156:159], v[60:63]\n"
        "ds_read_b64_tr_b16 v[86:87], v116 offset:8704\n"
        "s_setprio 1\n"
        "v_mul_f32 v8, v8, v24\n"
        "v_mul_f32 v9, v9, v25\n"
        "v_mul_f32 v10, v10, v26\n"
        "v_mul_f32 v11, v11, v27\n"
        "v_mfma_f32_16x16x32_bf16 v[72:75], v[92:95], v[152:155], v[72:75]\n"
        "ds_read_b64_tr_b16 v[88:89], v117 offset:8192\n"
        "v_exp_f32 v8, v8\n"
        "v_exp_f32 v9, v9\n"
        "v_mfma_f32_16x16x32_bf16 v[76:79], v[92:95], v[156:159], v[76:79]\n"
        "ds_read_b64_tr_b16 v[90:91], v117 offset:8704\n"
        "v_exp_f32 v10, v10\n"
        "v_exp_f32 v11, v11\n"
        "s_waitcnt lgkmcnt(4)\n"
        "v_mfma_f32_16x16x32_bf16 v[48:51], v[112:115], v[160:163], v[48:51]\n"
        "ds_read_b64_tr_b16 v[92:93], v116 offset:4096\n"
        "v_mul_f32 v40, v24, v40\n"
        "v_mul_f32 v41, v25, v41\n"
        "v_mul_f32 v42, v26, v42\n"
        "v_mul_f32 v43, v27, v43\n"
        "v_mfma_f32_16x16x32_bf16 v[52:55], v[112:115], v[164:167], v[52:55]\n"
        "ds_read_b64_tr_b16 v[94:95], v116 offset:4608\n"
        "v_add_f32 v8, 1.0, v8\n"
        "v_add_f32 v9, 1.0, v9\n"
        "v_add_f32 v10, 1.0, v10\n"
        "v_add_f32 v11, 1.0, v11\n"
        "v_mfma_f32_16x16x32_bf16 v[64:67], v[80:83], v[160:163], v[64:67]\n"
        "ds_read_b64_tr_b16 v[112:113], v117 offset:4096\n"
        "v_rcp_f32 v8, v8\n"
        "v_rcp_f32 v9, v9\n"
        "v_mfma_f32_16x16x32_bf16 v[68:71], v[80:83], v[164:167], v[68:71]\n"
        "ds_read_b64_tr_b16 v[114:115], v117 offset:4608\n"
        "v_rcp_f32 v10, v10\n"
        "v_rcp_f32 v11, v11\n"
        "s_waitcnt lgkmcnt(4)\n"
        "v_mfma_f32_16x16x32_bf16 v[56:59], v[84:87], v[160:163], v[56:59]\n"
        "ds_read_b64_tr_b16 v[80:81], v116 offset:12288\n"
        "v_mul_f32 v40, v40, v8\n"
        "v_mul_f32 v41, v41, v9\n"
        "v_mul_f32 v42, v42, v10\n"
        "v_mul_f32 v43, v43, v11\n"
        "v_mfma_f32_16x16x32_bf16 v[60:63], v[84:87], v[164:167], v[60:63]\n"
        "ds_read_b64_tr_b16 v[82:83], v116 offset:12800\n"
        "v_cvt_pk_bf16_f32 v148, v40, v41\n"
        "v_cvt_pk_bf16_f32 v149, v42, v43\n"
        "v_mul_f32 v8, v28, v28\n"
        "v_mul_f32 v9, v29, v29\n"
        "v_mfma_f32_16x16x32_bf16 v[72:75], v[88:91], v[160:163], v[72:75]\n"
        "ds_read_b64_tr_b16 v[84:85], v117 offset:12288\n"
        "v_mul_f32 v10, v30, v30\n"
        "v_mul_f32 v11, v31, v31\n"
        "v_fma_f32 v8, v8, s50, v122\n"
        "v_fma_f32 v9, v9, s50, v122\n"
        "v_mfma_f32_16x16x32_bf16 v[76:79], v[88:91], v[164:167], v[76:79]\n"
        "ds_read_b64_tr_b16 v[86:87], v117 offset:12800\n"
        "s_setprio 0\n"
        "v_fma_f32 v10, v10, s50, v122\n"
        "v_fma_f32 v11, v11, s50, v122\n"
        "v_mul_f32 v8, v8, v28\n"
        "v_mul_f32 v9, v9, v29\n"
        "s_waitcnt lgkmcnt(4)\n"
        "v_mfma_f32_16x16x32_bf16 v[48:51], v[92:95], v[168:171], v[48:51]\n"
        "s_waitcnt vmcnt(0) lgkmcnt(0)\n"
        "s_barrier\n"
        "s_cmp_lg_u32 s52, 0\n"
        "s_cbranch_scc1 LPA%=\n"
        "s_mov_b64 exec, 1\n"
        "v_mov_b32 v106, s48\n"
        "v_mov_b32 v107, s49\n"
        "buffer_store_dwordx2 v[106:107], off, s[28:31], s66 sc1\n"
        "s_mov_b64 exec, -1\n"
        "LPA%=:\n"
        "ds_read_b64_tr_b16 v[88:89], v118 offset:40960\n"
        "v_mul_f32 v10, v10, v30\n"
        "v_mul_f32 v11, v11, v31\n"
        "v_exp_f32 v8, v8\n"
        "v_mfma_f32_16x16x32_bf16 v[52:55], v[92:95], v[172:175], v[52:55]\n"
        "ds_read_b64_tr_b16 v[90:91], v118 offset:41472\n"
        "s_add_u32 m0, s53, 106496\n"
        "s_xor_b32 s57, s3, 5\n"
        "s_mul_i32 s58, s57, 229376\n"
        "buffer_load_dwordx4 v108, s[12:15], s58 offen lds\n"
        "s_setprio 3\n"
        "v_exp_f32 v9, v9\n"
        "v_exp_f32 v10, v10\n"
        "v_mfma_f32_16x16x32_bf16 v[64:67], v[112:115], v[168:171], v[64:67]\n"
        "ds_read_b64_tr_b16 v[92:93], v119 offset:40960\n"
        "v_exp_f32 v11, v11\n"
        "v_mul_f32 v44, v28, v44\n"
        "v_mul_f32 v45, v29, v45\n"
        "v_mfma_f32_16x16x32_bf16 v[68:71], v[112:115], v[172:175], v[68:71]\n"
        "ds_read_b64_tr_b16 v[94:95], v119 offset:41472\n"
        "s_add_u32 m0, s53, 114688\n"
        "s_nop 0\n"
        "buffer_load_dwordx4 v108, s[16:19], s58 offen lds\n"
        "v_mul_f32 v46, v30, v46\n"
        "v_mul_f32 v47, v31, v47\n"
        "v_add_f32 v8, 1.0, v8\n"
        "v_add_f32 v9, 1.0, v9\n"
        "v_mfma_f32_16x16x32_bf16 v[56:59], v[80:83], v[168:171], v[56:59]\n"
        "ds_read_b64_tr_b16 v[112:113], v120 offset:0\n"
        "v_add_f32 v10, 1.0, v10\n"
        "v_add_f32 v11, 1.0, v11\n"
        "v_rcp_f32 v8, v8\n"
        "v_mfma_f32_16x16x32_bf16 v[60:63], v[80:83], v[172:175], v[60:63]\n"
        "ds_read_b64_tr_b16 v[114:115], v120 offset:512\n"
        "s_add_u32 m0, s53, 49152\n"
        "s_xor_b32 s57, s3, 7\n"
        "s_mul_i32 s58, s57, 229376\n"
        "buffer_load_dwordx4 v108, s[12:15], s58 offen lds\n"
        "buffer_load_dwordx4 v[160:163], v124, s[4:7], s71 offen\n"
        "v_rcp_f32 v9, v9\n"
        "v_rcp_f32 v10, v10\n"
        "v_mfma_f32_16x16x32_bf16 v[72:75], v[84:87], v[168:171], v[72:75]\n"
        "ds_read_b64_tr_b16 v[80:81], v121 offset:0\n"
        "v_rcp_f32 v11, v11\n"
        "v_mul_f32 v44, v44, v8\n"
        "v_mul_f32 v45, v45, v9\n"
        "v_mfma_f32_16x16x32_bf16 v[76:79], v[84:87], v[172:175], v[76:79]\n"
        "ds_read_b64_tr_b16 v[82:83], v121 offset:512\n"
        "s_add_u32 m0, s53, 57344\n"
        "s_nop 0\n"
        "buffer_load_dwordx4 v108, s[16:19], s58 offen lds\n"
        "buffer_load_dwordx4 v[164:167], v124, s[36:39], 0 offen\n"
        "v_mul_f32 v46, v46, v10\n"
        "v_mul_f32 v47, v47, v11\n"
        "v_cvt_pk_bf16_f32 v150, v44, v45\n"
        "v_cvt_pk_bf16_f32 v151, v46, v47\n"
        "s_waitcnt lgkmcnt(4)\n"
        "v_mfma_f32_16x16x32_bf16 v[48:51], v[88:91], v[176:179], v[48:51]\n"
        "ds_read_b64_tr_b16 v[84:85], v118 offset:45056\n"
        "s_nop 0\n"
        "v_permlane16_swap_b32 v148, v150\n"
        "v_permlane16_swap_b32 v149, v151\n"
        "v_mfma_f32_16x16x32_bf16 v[52:55], v[88:91], v[180:183], v[52:55]\n"
        "ds_read_b64_tr_b16 v[86:87], v118 offset:45568\n"
        "v_mov_b64 v[168:169], 0\n"
        "v_mov_b64 v[170:171], 0\n"
        "s_nop 1\n"
        "v_mfma_f32_32x32x16_bf16 v[0:15], v[168:171], v[168:171], 0\n"
        "v_mfma_f32_16x16x32_bf16 v[64:67], v[92:95], v[176:179], v[64:67]\n"
        "ds_read_b64_tr_b16 v[88:89], v119 offset:45056\n"
        "v_mfma_f32_32x32x16_bf16 v[16:31], v[168:171], v[168:171], 0\n"
        "v_mfma_f32_16x16x32_bf16 v[68:71], v[92:95], v[180:183], v[68:71]\n"
        "ds_read_b64_tr_b16 v[90:91], v119 offset:45568\n"
        "s_waitcnt lgkmcnt(4)\n"
        "v_mfma_f32_16x16x32_bf16 v[56:59], v[112:115], v[176:179], v[56:59]\n"
        "ds_read_b64_tr_b16 v[92:93], v120 offset:4096\n"
        "v_mfma_f32_16x16x32_bf16 v[60:63], v[112:115], v[180:183], v[60:63]\n"
        "ds_read_b64_tr_b16 v[94:95], v120 offset:4608\n"
        "v_mfma_f32_16x16x32_bf16 v[72:75], v[80:83], v[176:179], v[72:75]\n"
        "ds_read_b64_tr_b16 v[112:113], v121 offset:4096\n"
        "v_mfma_f32_16x16x32_bf16 v[76:79], v[80:83], v[180:183], v[76:79]\n"
        "ds_read_b64_tr_b16 v[114:115], v121 offset:4608\n"
        "s_waitcnt lgkmcnt(4)\n"
        "v_mfma_f32_16x16x32_bf16 v[48:51], v[84:87], v[184:187], v[48:51]\n"
        "ds_read_b64_tr_b16 v[80:81], v118 offset:24576\n"
        "v_mfma_f32_16x16x32_bf16 v[52:55], v[84:87], v[188:191], v[52:55]\n"
        "ds_read_b64_tr_b16 v[82:83], v118 offset:25088\n"
        "v_mfma_f32_16x16x32_bf16 v[64:67], v[88:91], v[184:187], v[64:67]\n"
        "ds_read_b64_tr_b16 v[84:85], v119 offset:24576\n"
        "v_mfma_f32_16x16x32_bf16 v[68:71], v[88:91], v[188:191], v[68:71]\n"
        "ds_read_b64_tr_b16 v[86:87], v119 offset:25088\n"
        "s_waitcnt lgkmcnt(4)\n"
        "v_mfma_f32_16x16x32_bf16 v[56:59], v[92:95], v[184:187], v[56:59]\n"
        "ds_read_b64_tr_b16 v[88:89], v118 offset:32768\n"
        "v_mfma_f32_16x16x32_bf16 v[60:63], v[92:95], v[188:191], v[60:63]\n"
        "ds_read_b64_tr_b16 v[90:91], v118 offset:33280\n"
        "s_setprio 2\n"
        "v_mfma_f32_16x16x32_bf16 v[72:75], v[112:115], v[184:187], v[72:75]\n"
        "ds_read_b64_tr_b16 v[92:93], v119 offset:32768\n"
        "v_mfma_f32_16x16x32_bf16 v[76:79], v[112:115], v[188:191], v[76:79]\n"
        "ds_read_b64_tr_b16 v[94:95], v119 offset:33280\n"
        "s_waitcnt lgkmcnt(4)\n"
        "v_mfma_f32_16x16x32_bf16 v[48:51], v[80:83], v[192:195], v[48:51]\n"
        "ds_read_b64_tr_b16 v[112:113], v118 offset:28672\n"
        "v_mfma_f32_16x16x32_bf16 v[52:55], v[80:83], v[196:199], v[52:55]\n"
        "ds_read_b64_tr_b16 v[114:115], v118 offset:29184\n"
        "v_mfma_f32_16x16x32_bf16 v[64:67], v[84:87], v[192:195], v[64:67]\n"
        "ds_read_b64_tr_b16 v[80:81], v119 offset:28672\n"
        "v_mfma_f32_16x16x32_bf16 v[68:71], v[84:87], v[196:199], v[68:71]\n"
        "ds_read_b64_tr_b16 v[82:83], v119 offset:29184\n"
        "s_waitcnt lgkmcnt(4)\n"
        "v_mfma_f32_16x16x32_bf16 v[56:59], v[88:91], v[192:195], v[56:59]\n"
        "ds_read_b64_tr_b16 v[84:85], v118 offset:36864\n"
        "v_mfma_f32_16x16x32_bf16 v[60:63], v[88:91], v[196:199], v[60:63]\n"
        "ds_read_b64_tr_b16 v[86:87], v118 offset:37376\n"
        "v_mfma_f32_16x16x32_bf16 v[72:75], v[92:95], v[192:195], v[72:75]\n"
        "ds_read_b64_tr_b16 v[88:89], v119 offset:36864\n"
        "v_mfma_f32_16x16x32_bf16 v[76:79], v[92:95], v[196:199], v[76:79]\n"
        "ds_read_b64_tr_b16 v[90:91], v119 offset:37376\n"
        "s_setprio 1\n"
        "s_waitcnt lgkmcnt(4)\n"
        "v_mfma_f32_16x16x32_bf16 v[48:51], v[112:115], v[200:203], v[48:51]\n"
        "ds_read_b64_tr_b16 v[92:93], v116 offset:32768\n"
        "v_mfma_f32_16x16x32_bf16 v[52:55], v[112:115], v[204:207], v[52:55]\n"
        "ds_read_b64_tr_b16 v[94:95], v116 offset:33280\n"
        "v_mfma_f32_16x16x32_bf16 v[64:67], v[80:83], v[200:203], v[64:67]\n"
        "ds_read_b64_tr_b16 v[112:113], v116 offset:40960\n"
        "v_mfma_f32_16x16x32_bf16 v[68:71], v[80:83], v[204:207], v[68:71]\n"
        "ds_read_b64_tr_b16 v[114:115], v116 offset:41472\n"
        "s_waitcnt lgkmcnt(4)\n"
        "v_mfma_f32_16x16x32_bf16 v[56:59], v[84:87], v[200:203], v[56:59]\n"
        "ds_read_b64_tr_b16 v[80:81], v116 offset:36864\n"
        "v_mfma_f32_16x16x32_bf16 v[60:63], v[84:87], v[204:207], v[60:63]\n"
        "ds_read_b64_tr_b16 v[82:83], v116 offset:37376\n"
        "v_mfma_f32_16x16x32_bf16 v[72:75], v[88:91], v[200:203], v[72:75]\n"
        "ds_read_b64_tr_b16 v[84:85], v116 offset:45056\n"
        "v_mfma_f32_16x16x32_bf16 v[76:79], v[88:91], v[204:207], v[76:79]\n"
        "ds_read_b64_tr_b16 v[86:87], v116 offset:45568\n"
        "s_setprio 0\n"
        "s_waitcnt lgkmcnt(4)\n"
        "v_mfma_f32_16x16x32_bf16 v[48:51], v[92:95], v[208:211], v[48:51]\n"
        "s_waitcnt vmcnt(1) lgkmcnt(0)\n"
        "s_barrier\n"
        "ds_read_b64_tr_b16 v[88:89], v120 offset:8192\n"
        "v_mfma_f32_16x16x32_bf16 v[52:55], v[92:95], v[212:215], v[52:55]\n"
        "ds_read_b64_tr_b16 v[90:91], v120 offset:8704\n"
        "s_setprio 3\n"
        "v_mfma_f32_16x16x32_bf16 v[56:59], v[112:115], v[208:211], v[56:59]\n"
        "ds_read_b64_tr_b16 v[92:93], v120 offset:16384\n"
        "v_mfma_f32_16x16x32_bf16 v[60:63], v[112:115], v[212:215], v[60:63]\n"
        "ds_read_b64_tr_b16 v[94:95], v120 offset:16896\n"
        "v_mfma_f32_16x16x32_bf16 v[48:51], v[80:83], v[216:219], v[48:51]\n"
        "ds_read_b64_tr_b16 v[112:113], v120 offset:12288\n"
        "v_mfma_f32_16x16x32_bf16 v[52:55], v[80:83], v[220:223], v[52:55]\n"
        "ds_read_b64_tr_b16 v[114:115], v120 offset:12800\n"
        "s_waitcnt vmcnt(0)\n"
        "v_mov_b32 v109, 0\n"
        "v_dot2c_f32_bf16 v109, v160, v164\n"
        "v_dot2c_f32_bf16 v109, v161, v165\n"
        "v_mfma_f32_16x16x32_bf16 v[56:59], v[84:87], v[216:219], v[56:59]\n"
        "ds_read_b64_tr_b16 v[80:81], v120 offset:20480\n"
        "v_dot2c_f32_bf16 v109, v162, v166\n"
        "v_dot2c_f32_bf16 v109, v163, v167\n"
        "s_nop 2\n"
        "v_add_f32_dpp v109, v109, v109 row_shr:8 row_mask:0xf bank_mask:0xf bound_ctrl:1\n"
        "s_nop 1\n"
        "v_mfma_f32_16x16x32_bf16 v[60:63], v[84:87], v[220:223], v[60:63]\n"
        "ds_read_b64_tr_b16 v[82:83], v120 offset:20992\n"
        "v_add_f32_dpp v109, v109, v109 row_shr:4 row_mask:0xf bank_mask:0xf bound_ctrl:1\n"
        "s_nop 1\n"
        "v_add_f32_dpp v109, v109, v109 row_shr:2 row_mask:0xf bank_mask:0xf bound_ctrl:1\n"
        "s_nop 1\n"
        "v_add_f32_dpp v109, v109, v109 row_shr:1 row_mask:0xf bank_mask:0xf bound_ctrl:1\n"
        "s_waitcnt lgkmcnt(4)\n"
        "v_mfma_f32_16x16x32_bf16 v[48:51], v[88:91], v[224:227], v[48:51]\n"
        "ds_read_b64_tr_b16 v[84:85], v118 offset:0\n"
        "s_nop 1\n"
        "v_add_f32_dpp v109, v109, v109 row_bcast:15 row_mask:0xa bank_mask:0xf\n"
        "s_nop 1\n"
        "v_add_f32_dpp v109, v109, v109 row_bcast:31 row_mask:0xc bank_mask:0xf\n"
        "v_mul_f32 v109, 0xbfb8aa3b, v109\n"
        "v_mfma_f32_16x16x32_bf16 v[52:55], v[88:91], v[228:231], v[52:55]\n"
        "ds_read_b64_tr_b16 v[86:87], v118 offset:512\n"
        "v_exp_f32 v109, v109\n"
        "s_nop 0\n"
        "v_add_f32 v109, 1.0, v109\n"
        "v_rcp_f32 v160, v109\n"
        "v_mfma_f32_16x16x32_bf16 v[56:59], v[92:95], v[224:227], v[56:59]\n"
        "ds_read_b64_tr_b16 v[88:89], v118 offset:8192\n"
        "v_mfma_f32_16x16x32_bf16 v[60:63], v[92:95], v[228:231], v[60:63]\n"
        "ds_read_b64_tr_b16 v[90:91], v118 offset:8704\n"
        "s_waitcnt lgkmcnt(4)\n"
        "v_mfma_f32_16x16x32_bf16 v[48:51], v[112:115], v[232:235], v[48:51]\n"
        "ds_read_b64_tr_b16 v[92:93], v118 offset:4096\n"
        "v_mfma_f32_16x16x32_bf16 v[52:55], v[112:115], v[236:239], v[52:55]\n"
        "ds_read_b64_tr_b16 v[94:95], v118 offset:4608\n"
        "buffer_load_dwordx2 v[106:107], v123, s[28:31], 0 offen sc1\n"
        "v_mfma_f32_16x16x32_bf16 v[56:59], v[80:83], v[232:235], v[56:59]\n"
        "ds_read_b64_tr_b16 v[112:113], v118 offset:12288\n"
        "v_mfma_f32_16x16x32_bf16 v[60:63], v[80:83], v[236:239], v[60:63]\n"
        "ds_read_b64_tr_b16 v[114:115], v118 offset:12800\n"
        "s_waitcnt lgkmcnt(4)\n"
        "v_mfma_f32_16x16x32_bf16 v[48:51], v[84:87], v[240:243], v[48:51]\n"
        "ds_read_b64_tr_b16 v[80:81], v117 offset:32768\n"
        "v_mfma_f32_16x16x32_bf16 v[52:55], v[84:87], v[244:247], v[52:55]\n"
        "ds_read_b64_tr_b16 v[82:83], v117 offset:33280\n"
        "v_mfma_f32_16x16x32_bf16 v[56:59], v[88:91], v[240:243], v[56:59]\n"
        "ds_read_b64_tr_b16 v[84:85], v117 offset:40960\n"
        "v_mfma_f32_16x16x32_bf16 v[60:63], v[88:91], v[244:247], v[60:63]\n"
        "ds_read_b64_tr_b16 v[86:87], v117 offset:41472\n"
        "s_waitcnt lgkmcnt(4)\n"
        "v_mfma_f32_16x16x32_bf16 v[48:51], v[92:95], v[248:251], v[48:51]\n"
        "ds_read_b64_tr_b16 v[88:89], v117 offset:36864\n"
        "v_mfma_f32_16x16x32_bf16 v[52:55], v[92:95], v[252:255], v[52:55]\n"
        "ds_read_b64_tr_b16 v[90:91], v117 offset:37376\n"
        "v_mfma_f32_16x16x32_bf16 v[56:59], v[112:115], v[248:251], v[56:59]\n"
        "ds_read_b64_tr_b16 v[92:93], v117 offset:45056\n"
        "v_mfma_f32_16x16x32_bf16 v[60:63], v[112:115], v[252:255], v[60:63]\n"
        "ds_read_b64_tr_b16 v[94:95], v117 offset:45568\n"
        "s_waitcnt lgkmcnt(4)\n"
        "v_mfma_f32_16x16x32_bf16 v[64:67], v[80:83], v[208:211], v[64:67]\n"
        "ds_read_b64_tr_b16 v[112:113], v121 offset:8192\n"
        "v_mfma_f32_16x16x32_bf16 v[68:71], v[80:83], v[212:215], v[68:71]\n"
        "ds_read_b64_tr_b16 v[114:115], v121 offset:8704\n"
        "s_waitcnt vmcnt(0)\n"
        "v_cmp_ne_u64_e64 vcc, v[106:107], s[48:49]\n"
        "s_mov_b32 s77, 0x100000\n"
        "s_cmp_eq_u64 vcc, 0\n"
        "s_cbranch_scc1 LCAQ%=\n"
        "LCAP%=:\n"
        "s_sleep 1\n"
        "buffer_load_dwordx2 v[106:107], v123, s[28:31], 0 offen sc1\n"
        "s_sub_u32 s77, s77, 1\n"
        "s_cmp_eq_u32 s77, 0\n"
        "s_waitcnt vmcnt(0)\n"
        "s_cbranch_scc1 LCAQ%=\n"
        "v_cmp_ne_u64_e64 vcc, v[106:107], s[48:49]\n"
        "s_nop 0\n"
        "s_cmp_eq_u64 vcc, 0\n"
        "s_cbranch_scc0 LCAP%=\n"
        "LCAQ%=:\n"
        "buffer_load_dwordx4 v[128:131], v124, s[28:31], s69 offen sc1\n"
        "s_setprio 2\n"
        "v_mfma_f32_16x16x32_bf16 v[72:75], v[84:87], v[208:211], v[72:75]\n"
        "ds_read_b64_tr_b16 v[80:81], v121 offset:16384\n"
        "v_mfma_f32_16x16x32_bf16 v[76:79], v[84:87], v[212:215], v[76:79]\n"
        "ds_read_b64_tr_b16 v[82:83], v121 offset:16896\n"
        "buffer_load_dwordx4 v[132:135], v124, s[28:31], s70 offen sc1\n"
        "s_waitcnt lgkmcnt(4)\n"
        "v_mfma_f32_16x16x32_bf16 v[64:67], v[88:91], v[216:219], v[64:67]\n"
        "ds_read_b64_tr_b16 v[84:85], v121 offset:12288\n"
        "v_mfma_f32_16x16x32_bf16 v[68:71], v[88:91], v[220:223], v[68:71]\n"
        "ds_read_b64_tr_b16 v[86:87], v121 offset:12800\n"
        "s_mov_b32 m0, s54\n"
        "s_xor_b32 s57, s94, 0\n"
        "buffer_load_dwordx4 v125, s[28:31], s57 offen sc1 lds\n"
        "v_mul_f32 v172, v48, v48\n"
        "v_mul_f32 v173, v49, v49\n"
        "v_mul_f32 v174, v50, v50\n"
        "v_mul_f32 v175, v51, v51\n"
        "v_mfma_f32_16x16x32_bf16 v[72:75], v[92:95], v[216:219], v[72:75]\n"
        "ds_read_b64_tr_b16 v[88:89], v121 offset:20480\n"
        "v_fma_f32 v172, v172, s50, v122\n"
        "v_fma_f32 v173, v173, s50, v122\n"
        "v_fma_f32 v174, v174, s50, v122\n"
        "v_fma_f32 v175, v175, s50, v122\n"
        "v_mfma_f32_16x16x32_bf16 v[76:79], v[92:95], v[220:223], v[76:79]\n"
        "ds_read_b64_tr_b16 v[90:91], v121 offset:20992\n"
        "s_mov_b32 m0, s54\n"
        "s_xor_b32 s57, s94, 0\n"
        "buffer_load_dwordx4 v125, s[28:31], s57 offen offset:1024 sc1 lds\n"
        "v_mul_f32 v172, v172, v48\n"
        "v_mul_f32 v173, v173, v49\n"
        "v_mul_f32 v174, v174, v50\n"
        "v_mul_f32 v175, v175, v51\n"
        "s_waitcnt lgkmcnt(4)\n"
        "v_mfma_f32_16x16x32_bf16 v[64:67], v[112:115], v[224:227], v[64:67]\n"
        "ds_read_b64_tr_b16 v[92:93], v119 offset:0\n"
        "v_exp_f32 v172, v172\n"
        "v_exp_f32 v173, v173\n"
        "v_mfma_f32_16x16x32_bf16 v[68:71], v[112:115], v[228:231], v[68:71]\n"
        "ds_read_b64_tr_b16 v[94:95], v119 offset:512\n"
        "s_add_u32 m0, s54, 16384\n"
        "s_xor_b32 s57, s94, 16384\n"
        "buffer_load_dwordx4 v125, s[28:31], s57 offen sc1 lds\n"
        "v_exp_f32 v174, v174\n"
        "v_exp_f32 v175, v175\n"
        "v_mfma_f32_16x16x32_bf16 v[72:75], v[80:83], v[224:227], v[72:75]\n"
        "ds_read_b64_tr_b16 v[112:113], v119 offset:8192\n"
        "v_mul_f32 v56, v48, v56\n"
        "v_mul_f32 v57, v49, v57\n"
        "v_mul_f32 v58, v50, v58\n"
        "v_mul_f32 v59, v51, v59\n"
        "v_mfma_f32_16x16x32_bf16 v[76:79], v[80:83], v[228:231], v[76:79]\n"
        "ds_read_b64_tr_b16 v[114:115], v119 offset:8704\n"
        "s_add_u32 m0, s54, 16384\n"
        "s_xor_b32 s57, s94, 16384\n"
        "buffer_load_dwordx4 v125, s[28:31], s57 offen offset:1024 sc1 lds\n"
        "v_add_f32 v172, 1.0, v172\n"
        "v_add_f32 v173, 1.0, v173\n"
        "v_add_f32 v174, 1.0, v174\n"
        "v_add_f32 v175, 1.0, v175\n"
        "s_waitcnt lgkmcnt(4)\n"
        "v_mfma_f32_16x16x32_bf16 v[64:67], v[84:87], v[232:235], v[64:67]\n"
        "ds_read_b64_tr_b16 v[80:81], v119 offset:4096\n"
        "v_rcp_f32 v172, v172\n"
        "v_rcp_f32 v173, v173\n"
        "v_mfma_f32_16x16x32_bf16 v[68:71], v[84:87], v[236:239], v[68:71]\n"
        "ds_read_b64_tr_b16 v[82:83], v119 offset:4608\n"
        "s_setprio 1\n"
        "v_rcp_f32 v174, v174\n"
        "v_rcp_f32 v175, v175\n"
        "v_mfma_f32_16x16x32_bf16 v[72:75], v[88:91], v[232:235], v[72:75]\n"
        "ds_read_b64_tr_b16 v[84:85], v119 offset:12288\n"
        "v_mul_f32 v56, v56, v172\n"
        "v_mul_f32 v57, v57, v173\n"
        "v_mul_f32 v58, v58, v174\n"
        "v_mul_f32 v59, v59, v175\n"
        "v_mfma_f32_16x16x32_bf16 v[76:79], v[88:91], v[236:239], v[76:79]\n"
        "ds_read_b64_tr_b16 v[86:87], v119 offset:12800\n"
        "v_cvt_pk_bf16_f32 v152, v56, v57\n"
        "v_cvt_pk_bf16_f32 v153, v58, v59\n"
        "v_mul_f32 v172, v52, v52\n"
        "v_mul_f32 v173, v53, v53\n"
        "s_waitcnt lgkmcnt(4)\n"
        "v_mfma_f32_16x16x32_bf16 v[64:67], v[92:95], v[240:243], v[64:67]\n"
        "v_mul_f32 v174, v54, v54\n"
        "v_mul_f32 v175, v55, v55\n"
        "v_fma_f32 v172, v172, s50, v122\n"
        "v_fma_f32 v173, v173, s50, v122\n"
        "v_mfma_f32_16x16x32_bf16 v[68:71], v[92:95], v[244:247], v[68:71]\n"
        "v_fma_f32 v174, v174, s50, v122\n"
        "v_fma_f32 v175, v175, s50, v122\n"
        "v_mul_f32 v172, v172, v52\n"
        "v_mul_f32 v173, v173, v53\n"
        "v_mfma_f32_16x16x32_bf16 v[72:75], v[112:115], v[240:243], v[72:75]\n"
        "v_mul_f32 v174, v174, v54\n"
        "v_mul_f32 v175, v175, v55\n"
        "v_exp_f32 v172, v172\n"
        "v_mfma_f32_16x16x32_bf16 v[76:79], v[112:115], v[244:247], v[76:79]\n"
        "v_exp_f32 v173, v173\n"
        "v_exp_f32 v174, v174\n"
        "s_waitcnt lgkmcnt(0)\n"
        "v_mfma_f32_16x16x32_bf16 v[64:67], v[80:83], v[248:251], v[64:67]\n"
        "v_exp_f32 v175, v175\n"
        "v_mul_f32 v60, v52, v60\n"
        "v_mul_f32 v61, v53, v61\n"
        "v_mfma_f32_16x16x32_bf16 v[68:71], v[80:83], v[252:255], v[68:71]\n"
        "s_setprio 0\n"
        "v_mul_f32 v62, v54, v62\n"
        "v_mul_f32 v63, v55, v63\n"
        "v_add_f32 v172, 1.0, v172\n"
        "v_add_f32 v173, 1.0, v173\n"
        "v_mfma_f32_16x16x32_bf16 v[72:75], v[84:87], v[248:251], v[72:75]\n"
        "v_add_f32 v174, 1.0, v174\n"
        "v_add_f32 v175, 1.0, v175\n"
        "v_rcp_f32 v172, v172\n"
        "v_mfma_f32_16x16x32_bf16 v[76:79], v[84:87], v[252:255], v[76:79]\n"
        "v_rcp_f32 v173, v173\n"
        "v_rcp_f32 v174, v174\n"
        "v_rcp_f32 v175, v175\n"
        "v_mul_f32 v60, v60, v172\n"
        "v_mul_f32 v61, v61, v173\n"
        "v_mul_f32 v62, v62, v174\n"
        "v_mul_f32 v63, v63, v175\n"
        "v_cvt_pk_bf16_f32 v154, v60, v61\n"
        "v_cvt_pk_bf16_f32 v155, v62, v63\n"
        "s_nop 0\n"
        "v_permlane16_swap_b32 v152, v154\n"
        "v_permlane16_swap_b32 v153, v155\n"
        "v_pk_mul_f32 v[172:173], v[64:65], v[64:65]\n"
        "v_pk_mul_f32 v[174:175], v[66:67], v[66:67]\n"
        "v_pk_fma_f32 v[172:173], v[172:173], s[50:51], v[122:123] op_sel_hi:[1,0,0]\n"
        "v_pk_fma_f32 v[174:175], v[174:175], s[50:51], v[122:123] op_sel_hi:[1,0,0]\n"
        "v_pk_mul_f32 v[172:173], v[172:173], v[64:65]\n"
        "v_pk_mul_f32 v[174:175], v[174:175], v[66:67]\n"
        "v_exp_f32 v172, v172\n"
        "v_exp_f32 v173, v173\n"
        "v_exp_f32 v174, v174\n"
        "v_exp_f32 v175, v175\n"
        "v_pk_mul_f32 v[72:73], v[64:65], v[72:73]\n"
        "v_pk_mul_f32 v[74:75], v[66:67], v[74:75]\n"
        "v_pk_add_f32 v[172:173], v[172:173], 1.0 op_sel_hi:[1,0]\n"
        "v_pk_add_f32 v[174:175], v[174:175], 1.0 op_sel_hi:[1,0]\n"
        "v_rcp_f32 v172, v172\n"
        "v_rcp_f32 v173, v173\n"
        "v_rcp_f32 v174, v174\n"
        "v_rcp_f32 v175, v175\n"
        "v_pk_mul_f32 v[72:73], v[72:73], v[172:173]\n"
        "v_pk_mul_f32 v[74:75], v[74:75], v[174:175]\n"
        "v_cvt_pk_bf16_f32 v156, v72, v73\n"
        "v_cvt_pk_bf16_f32 v157, v74, v75\n"
        "v_pk_mul_f32 v[172:173], v[68:69], v[68:69]\n"
        "v_pk_mul_f32 v[174:175], v[70:71], v[70:71]\n"
        "v_pk_fma_f32 v[172:173], v[172:173], s[50:51], v[122:123] op_sel_hi:[1,0,0]\n"
        "v_pk_fma_f32 v[174:175], v[174:175], s[50:51], v[122:123] op_sel_hi:[1,0,0]\n"
        "v_pk_mul_f32 v[172:173], v[172:173], v[68:69]\n"
        "v_pk_mul_f32 v[174:175], v[174:175], v[70:71]\n"
        "v_exp_f32 v172, v172\n"
        "v_exp_f32 v173, v173\n"
        "v_exp_f32 v174, v174\n"
        "v_exp_f32 v175, v175\n"
        "v_pk_mul_f32 v[76:77], v[68:69], v[76:77]\n"
        "v_pk_mul_f32 v[78:79], v[70:71], v[78:79]\n"
        "v_pk_add_f32 v[172:173], v[172:173], 1.0 op_sel_hi:[1,0]\n"
        "s_waitcnt vmcnt(0) lgkmcnt(0)\n"
        "s_barrier\n"
        "ds_read_b128 v[140:143], v124\n"
        "ds_read_b128 v[104:107], v97\n"
        "LEND%=:\n"
        "s_branch LATT%=\n"
                 : "+{v0}"(tid), "+{s[0:1]}"(args), "+{s2}"(bid)
                 : "i"((__attribute__((address_space(3))) char*)smem)
                 : "v1", "v2", "v3", "v4", "v5", "v6", "v7", "v8", "v9", "v10", "v11", "v12", "v13", "v14", "v15", "v16", "v17", "v18", "v19", "v20", "v21", "v22",
                   "v23", "v24", "v25", "v26", "v27", "v28", "v29", "v30", "v31", "v32", "v33", "v34", "v35", "v36", "v37", "v38", "v39", "v40", "v41", "v42", "v43",
                   "v44", "v45", "v46", "v47", "v48", "v49", "v50", "v51", "v52", "v53", "v54", "v55", "v56", "v57", "v58", "v59", "v60", "v61", "v62", "v63", "v64",
                   "v65", "v66", "v67", "v68", "v69", "v70", "v71", "v72", "v73", "v74", "v75", "v76", "v77", "v78", "v79", "v80", "v81", "v82", "v83", "v84", "v85",
                   "v86", "v87", "v88", "v89", "v90", "v91", "v92", "v93", "v94", "v95", "v96", "v97", "v98", "v99", "v100", "v101", "v102", "v103", "v104", "v105",
                   "v106", "v107", "v108", "v109", "v110", "v111", "v112", "v113", "v114", "v115", "v116", "v117", "v118", "v119", "v120", "v121", "v122", "v123",
                   "v124", "v125", "v126", "v127", "v128", "v129", "v130", "v131", "v132", "v133", "v134", "v135", "v136", "v137", "v138", "v139", "v140", "v141",
                   "v142", "v143", "v144", "v145", "v146", "v147", "v148", "v149", "v150", "v151", "v152", "v153", "v154", "v155", "v156", "v157", "v158", "v159",
                   "v160", "v161", "v162", "v163", "v164", "v165", "v166", "v167", "v168", "v169", "v170", "v171", "v172", "v173", "v174", "v175", "v176", "v177",
                   "v178", "v179", "v180", "v181", "v182", "v183", "v184", "v185", "v186", "v187", "v188", "v189", "v190", "v191", "v192", "v193", "v194", "v195",
                   "v196", "v197", "v198", "v199", "v200", "v201", "v202", "v203", "v204", "v205", "v206", "v207", "v208", "v209", "v210", "v211", "v212", "v213",
                   "v214", "v215", "v216", "v217", "v218", "v219", "v220", "v221", "v222", "v223", "v224", "v225", "v226", "v227", "v228", "v229", "v230", "v231",
                   "v232", "v233", "v234", "v235", "v236", "v237", "v238", "v239", "v240", "v241", "v242", "v243", "v244", "v245", "v246", "v247", "v248", "v249",
                   "v250", "v251", "v252", "v253", "v254", "v255", "s3", "s4", "s5", "s6", "s7", "s8", "s9", "s10", "s11", "s12", "s13", "s14", "s15", "s16", "s17",
                   "s18", "s19", "s20", "s21", "s22", "s23", "s24", "s25", "s26", "s27", "s28", "s29", "s30", "s31", "s32", "s33", "s34", "s35", "s36", "s37", "s38",
                   "s39", "s40", "s41", "s42", "s43", "s44", "s45", "s46", "s47", "s48", "s49", "s50", "s51", "s52", "s53", "s54", "s55", "s56", "s57", "s58", "s59",
                   "s60", "s61", "s62", "s63", "s64", "s65", "s66", "s67", "s68", "s69", "s70", "s71", "s72", "s73", "s74", "s75", "s76", "s77", "s78", "s79", "s80",
                   "s81", "s82", "s83", "s84", "s85", "s86", "s87", "s88", "s89", "s90", "s91", "s92", "s93", "s94", "s95", "s96", "s97", "s98", "s99",
                   "vcc", "scc", "memory");
}

extern "C" void kernel_launch(void* const* d_in, const int* in_sizes, int n_in, void* d_out, int out_size, void* d_ws, size_t ws_size,
                              hipStream_t stream) {
    const unsigned F = 0x00020000u;
    Args a = {{{d_in[0], 2048u * 1024u, F}, {d_in[1], 512u * 3072u, F}, {d_in[4], 512u * 3584u, F}, {d_in[5], 512u * 3584u, F}, {d_in[6], 1792u * 1024u, F},
               {d_in[2], 512u * 1024u, F}, {d_ws, WS_USED, F}, {d_out, 2048u * 1024u, F}, {d_in[7], 1024u, F}}};
    gated_mix_fused<<<256, 512, 0, stream>>>(a);
}
```
